```python
import torch

def _softmax(x, axis=-1):
    return torch.softmax(x, dim=axis)
def _gelu(x):
    return torch.nn.functional.gelu(x, approximate="tanh")
def _silu(x):
    return torch.nn.functional.silu(x)
def _sigmoid(x):
    return torch.sigmoid(x)
def _layernorm(x, axis=-1, eps=1e-5):
    m = x.mean(dim=axis, keepdim=True)
    v = x.var(dim=axis, keepdim=True, unbiased=False)
    return (x - m) / torch.sqrt(v + eps)
def _scatter_add(tgt, idx, src, axis=0):
    return tgt.clone().index_add_(axis, idx, src)
def _segment_sum(src, seg, n):
    out = torch.zeros((n,) + src.shape[1:], dtype=src.dtype)
    return out.index_add_(0, seg, src)
def _pad(x, pw):
    flat = tuple(v for pair in reversed(pw) for v in pair)
    return torch.nn.functional.pad(x, flat)

kernel_name = 'recurrent_scan__gqa_block_cached__dense_activation_relu__decode'

def setup_inputs():

    _w = (lambda a: (lambda t: t.to(torch.bfloat16) if t.is_floating_point() else t)(torch.as_tensor(a)))
    def _A_setup():
        B, Sq, D = 1, 1, 512
        Hq, Hkv, Sk, Dh = 32, 8, 65536, 128
        assert Hq % Hkv == 0, (Hq, Hkv)
        return {
            "x": _w((lambda *s: torch.randn(*s, dtype=torch.bfloat16) * 0.05)(B, Sq, D)),
            "k_cache": _w((lambda *s: torch.randn(*s, dtype=torch.bfloat16) * 0.8)(B, Hkv, Sk, Dh)),
            "v_cache": _w((lambda *s: torch.randn(*s, dtype=torch.bfloat16) * 0.8)(B, Hkv, Sk, Dh)),
            "wq": _w((lambda *s: torch.randn(*s, dtype=torch.bfloat16) * 0.8)(D, Hq * Dh) * (D ** -0.5)),
            "wo": _w((lambda *s: torch.randn(*s, dtype=torch.bfloat16) * 0.8)(Hq * Dh, D) * ((Hq * Dh) ** -0.5)),
        }
    _A_in = _A_setup()

    def _B_setup():
        B, S, D, Dff = 1, 1, 512, 1792
        return {
            "x": _w((lambda *s: torch.randn(*s, dtype=torch.bfloat16) * 0.05)(B, S, D)),
            "w1": _w((lambda *s: torch.randn(*s, dtype=torch.bfloat16) * 0.2)(D, Dff)),
            "w2": _w((lambda *s: torch.randn(*s, dtype=torch.bfloat16) * 0.2)(Dff, D)),
        }
    _B_in = _B_setup()

    return {
        "A_x": _A_in["x"],
        "A_k_cache": _A_in["k_cache"],
        "A_v_cache": _A_in["v_cache"],
        "A_wq": _A_in["wq"],
        "A_wo": _A_in["wo"],
        "B_x": _B_in["x"],
        "B_w1": _B_in["w1"],
        "B_w2": _B_in["w2"],
    }

def reference(A_x, A_k_cache, A_v_cache, A_wq, A_wo, B_x, B_w1, B_w2):
    def _A_ref(x, k_cache, v_cache, wq, wo):
        B, Sq, D = x.shape
        Hq, Dh = 32, 128
        Hkv = k_cache.shape[1]
        rep = Hq // Hkv
        q = (lambda x, *d: x.permute(*d))((x @ wq).reshape(B, Sq, Hq, Dh), 0, 2, 1, 3)
        k = (lambda x, n, axis: torch.repeat_interleave(x, n, dim=axis))(k_cache, rep, axis=1)
        v = (lambda x, n, axis: torch.repeat_interleave(x, n, dim=axis))(v_cache, rep, axis=1)
        scores = torch.einsum("bhqd,bhkd->bhqk", q, k) * (Dh ** -0.5)
        p = _softmax(scores, axis=-1)
        o = torch.einsum("bhqk,bhkd->bhqd", p, v)
        return (lambda x, *d: x.permute(*d))(o, 0, 2, 1, 3).reshape(B, Sq, Hq * Dh) @ wo

    def _B_ref(x, w1, w2):
        h = torch.einsum("bsd,df->bsf", x, w1)
        zero = torch.zeros_like(h)
        h = torch.maximum(h, zero)
        return torch.einsum("bsf,fd->bsd", h, w2)

    x = A_x
    xn = _layernorm(x, axis=-1)
    s = _A_ref(xn, A_k_cache, A_v_cache, A_wq, A_wo)
    g = _silu(_B_ref(xn, B_w1, B_w2))
    return x + s * g
```

```cpp
#include <hip/hip_runtime.h>
#include <hip/hip_bf16.h>
#include <stdint.h>

#define LAS __attribute__((address_space(3)))
#define GAS __attribute__((address_space(1)))
typedef unsigned u32x4 __attribute__((ext_vector_type(4)));
typedef unsigned u32x2 __attribute__((ext_vector_type(2)));
typedef int i32x4 __attribute__((ext_vector_type(4)));
typedef float f32x4 __attribute__((ext_vector_type(4)));
typedef __bf16 bf16x8 __attribute__((ext_vector_type(8)));
typedef __bf16 bf16x4 __attribute__((ext_vector_type(4)));
typedef short s16x4 __attribute__((ext_vector_type(4)));

namespace {

constexpr int DM = 512, HKV = 8, SK = 65536, DH = 128, DFF = 1792;
constexpr int NWG = 256, NTHR = 320;
constexpr int NFILL = 128;
constexpr int RING = 16, SLOT = 8192;
constexpr int NFFN = DFF / 16;
constexpr unsigned KV_BYTES = (unsigned)HKV * SK * DH * 2;

constexpr int L_RING = 0;
constexpr int L_CTRL = RING * SLOT;
constexpr int L_XN = L_CTRL + 256;
constexpr int L_Q = L_XN + 2048;
constexpr int L_RED = L_Q + 1024;
constexpr int L_MRG = L_RED + 1024;
constexpr int L_ML = L_MRG + 8192;
constexpr int L_END = L_ML + 256;

constexpr unsigned W_EPOCH = 0;
constexpr unsigned W_Q = 4096;
constexpr unsigned W_ML = W_Q + 8 * 128 * 16;
constexpr unsigned W_O = W_ML + 8 * 4 * 32 * 16;
constexpr unsigned W_S = W_O + 8 * 4 * 32 * 64 * 16;
constexpr unsigned W_F = W_S + 32 * 256 * 8 * 16;
constexpr unsigned W_END = W_F + 32 * NFFN * 8 * 16;

constexpr unsigned SPIN_CAP = 1u << 20;

__device__ __forceinline__ float bf2f(unsigned short b) { return __builtin_bit_cast(float, (unsigned)b << 16); }
__device__ __forceinline__ float bflo(unsigned u) { return __builtin_bit_cast(float, u << 16); }
__device__ __forceinline__ float bfhi(unsigned u) { return __builtin_bit_cast(float, u & 0xffff0000u); }
__device__ __forceinline__ unsigned short f2bf(float f) { return __builtin_bit_cast(unsigned short, (__bf16)f); }
__device__ __forceinline__ float rbf(float f) { return bf2f(f2bf(f)); }
__device__ __forceinline__ unsigned pack2(float lo, float hi) { return (unsigned)f2bf(lo) | ((unsigned)f2bf(hi) << 16); }

template <int CTRL> __device__ __forceinline__ float dpp(float x) {
  return __builtin_bit_cast(float, __builtin_amdgcn_mov_dpp(__builtin_bit_cast(int, x), CTRL, 0xf, 0xf, true));
}
constexpr int XOR1 = 0xB1, XOR2 = 0x4E, ROR4 = 0x124, ROR8 = 0x128;
__device__ __forceinline__ float xrow_sum(float x) {
  auto s = __builtin_amdgcn_permlane16_swap(__float_as_uint(x), __float_as_uint(x), false, false);
  x = __uint_as_float(s[0]) + __uint_as_float(s[1]);
  auto t = __builtin_amdgcn_permlane32_swap(__float_as_uint(x), __float_as_uint(x), false, false);
  return __uint_as_float(t[0]) + __uint_as_float(t[1]);
}
__device__ __forceinline__ float xrow_max(float x) {
  auto s = __builtin_amdgcn_permlane16_swap(__float_as_uint(x), __float_as_uint(x), false, false);
  x = fmaxf(__uint_as_float(s[0]), __uint_as_float(s[1]));
  auto t = __builtin_amdgcn_permlane32_swap(__float_as_uint(x), __float_as_uint(x), false, false);
  return fmaxf(__uint_as_float(t[0]), __uint_as_float(t[1]));
}
__device__ __forceinline__ float wave_sum(float x) {
  x += dpp<XOR1>(x); x += dpp<XOR2>(x); x += dpp<ROR4>(x); x += dpp<ROR8>(x);
  return xrow_sum(x);
}
__device__ __forceinline__ float half_sum_even(float x) {
  x += dpp<XOR2>(x); x += dpp<ROR4>(x); x += dpp<ROR8>(x);
  return xrow_sum(x);
}

__device__ __forceinline__ u32x4 ld_nt16(const void* p) { return __builtin_nontemporal_load((const GAS u32x4*)p); }
__device__ __forceinline__ unsigned ld_nt4(const void* p) { return __builtin_nontemporal_load((const GAS unsigned*)p); }

__device__ __forceinline__ void st_gran(__amdgpu_buffer_rsrc_t ws, unsigned off, unsigned v0, unsigned v1, unsigned tlo, unsigned thi) {
  u32x4 g = {v0, tlo, v1, thi};
  __builtin_amdgcn_raw_buffer_store_b128(g, ws, off, 0, 16);
}
__device__ __forceinline__ u32x4 ld_gran(__amdgpu_buffer_rsrc_t ws, unsigned off) {
  return __builtin_amdgcn_raw_buffer_load_b128(ws, off, 0, 16);
}

__device__ __forceinline__ void cons_sync(volatile LAS unsigned* cnt, unsigned target, int lane) {
  asm volatile("s_waitcnt lgkmcnt(0)" ::: "memory");
  if (lane == 0) __hip_atomic_fetch_add((LAS unsigned*)cnt, 1u, __ATOMIC_RELAXED, __HIP_MEMORY_SCOPE_WORKGROUP);
  for (unsigned spins = 0; *cnt < target && spins < SPIN_CAP; ++spins) {}
  asm volatile("" ::: "memory");
}

__device__ __forceinline__ void slice_dot(const u32x4 (&w)[4], f32x4 xn, float (&acc)[8]) {
#pragma unroll
  for (int k = 0; k < 8; ++k) acc[k] = 0.f;
#pragma unroll
  for (int i = 0; i < 4; ++i) {
#pragma unroll
    for (int k = 0; k < 4; ++k) {
      acc[2 * k] = fmaf(xn[i], bflo(w[i][k]), acc[2 * k]);
      acc[2 * k + 1] = fmaf(xn[i], bfhi(w[i][k]), acc[2 * k + 1]);
    }
  }
#pragma unroll
  for (int k = 0; k < 8; ++k) acc[k] = half_sum_even(acc[k]);
}

__global__ void __launch_bounds__(NTHR, 2)
fused_decode_kernel(const unsigned short* __restrict__ x, const unsigned short* __restrict__ kc, const unsigned short* __restrict__ vc,
                    const unsigned short* __restrict__ wq, const unsigned short* __restrict__ wo, const unsigned short* __restrict__ w1,
                    const unsigned short* __restrict__ w2, unsigned short* __restrict__ out, unsigned char* __restrict__ wsp) {
  extern __shared__ __attribute__((aligned(16))) unsigned char lds[];
  LAS unsigned char* L = (LAS unsigned char*)lds;
  volatile LAS unsigned* ctrl = (volatile LAS unsigned*)(L + L_CTRL);
  const int w = blockIdx.x, g = w & 7, s = w >> 3;
  const int wave = __builtin_amdgcn_readfirstlane(threadIdx.x >> 6), lane = threadIdx.x & 63;

  if (wave == 0) {
    const unsigned long long kb = (unsigned long long)kc, vb = (unsigned long long)vc;
    const i32x4 sk = {(int)kb, (int)(kb >> 32), (int)KV_BYTES, 0x00020000};
    const i32x4 sv = {(int)vb, (int)(vb >> 32), (int)KV_BYTES, 0x00020000};
    const unsigned base = (unsigned)g * (SK * DH * 2) + (unsigned)s * (NFILL * 4096);
    const unsigned r4 = lane >> 4, p = lane & 15;
    const unsigned k0 = r4 * 256 + 16 * (p ^ (0 + r4)), k1 = r4 * 256 + 16 * (p ^ (4 + r4));
    const unsigned k2 = r4 * 256 + 16 * (p ^ (8 + r4)), k3 = r4 * 256 + 16 * (p ^ (12 + r4));
    const unsigned v0 = r4 * 256 + 16 * (p ^ (r4 << 1)), v1 = r4 * 256 + 16 * (p ^ ((4 + r4) << 1));
    const unsigned ring0 = (unsigned)(size_t)(L + L_RING);
    auto issue = [&](int j) {
      const unsigned m0k = ring0 + (unsigned)(j & (RING - 1)) * SLOT, so = base + (unsigned)j * 4096;
      asm volatile(
          "s_mov_b32 m0, %0\n\ts_nop 0\n\t"
          "buffer_load_dwordx4 %2, %8, %10 offen nt lds\n\t"
          "buffer_load_dwordx4 %3, %8, %10 offen offset:1024 nt lds\n\t"
          "buffer_load_dwordx4 %4, %8, %10 offen offset:2048 nt lds\n\t"
          "buffer_load_dwordx4 %5, %8, %10 offen offset:3072 nt lds\n\t"
          "s_mov_b32 m0, %1\n\ts_nop 0\n\t"
          "buffer_load_dwordx4 %6, %9, %10 offen nt lds\n\t"
          "buffer_load_dwordx4 %7, %9, %10 offen offset:1024 nt lds\n\t"
          "buffer_load_dwordx4 %6, %9, %10 offen offset:2048 nt lds\n\t"
          "buffer_load_dwordx4 %7, %9, %10 offen offset:3072 nt lds"
          :: "s"(m0k), "s"(m0k + 4096), "v"(k0), "v"(k1), "v"(k2), "v"(k3), "v"(v0), "v"(v1), "s"(sk), "s"(sv), "s"(so)
          : "memory");
    };
    issue(0); issue(1);
    if (lane < 16) ctrl[lane] = 0u;
    asm volatile("s_waitcnt lgkmcnt(0)" ::: "memory");
    __builtin_amdgcn_s_barrier();
    int pub = 0;
    for (int j = 2; j < NFILL; ++j) {
      if (j >= RING) {
        const unsigned need = (unsigned)((j - RING) >> 2) + 1u;
        volatile LAS unsigned* dn = ctrl + 4 + ((j - RING) & 3);
        if (*dn < need) {
          asm volatile("s_waitcnt vmcnt(0)" ::: "memory");
          pub = j;
          ctrl[0] = (unsigned)pub;
          for (unsigned spins = 0; *dn < need && spins < SPIN_CAP; ++spins) __builtin_amdgcn_s_sleep(1);
        }
        asm volatile("" ::: "memory");
      }
      issue(j);
      if (j + 1 - pub > 2) {
        asm volatile("s_waitcnt vmcnt(16)" ::: "memory");
        pub = j - 1;
        ctrl[0] = (unsigned)pub;
      }
    }
    asm volatile("s_waitcnt vmcnt(8)" ::: "memory");
    ctrl[0] = NFILL - 1;
    asm volatile("s_waitcnt vmcnt(0)" ::: "memory");
    ctrl[0] = NFILL;
    return;
  }

  const int cw = wave - 1, t = threadIdx.x - 64;
  const unsigned long long wsb = (unsigned long long)wsp;
  const __amdgpu_buffer_rsrc_t ws = __builtin_amdgcn_make_buffer_rsrc((void*)wsp, 0, (int)W_END, 0x00020000);
  (void)wsb;
  const int c = g * 32 + s;
  const bool ffn = s < 14;
  const int job = g * 14 + s;

  const u32x2 ep = __builtin_amdgcn_raw_buffer_load_b64(ws, W_EPOCH, 0, 16);
  const u32x4 xr = *(const GAS u32x4*)(x + 8 * lane);
  const int half = lane & 1, row0 = 128 * cw + 4 * (lane >> 1);
  u32x4 wqr[4], w1r[4];
#pragma unroll
  for (int i = 0; i < 4; ++i) wqr[i] = ld_nt16(wq + (size_t)(row0 + i) * 4096 + g * 512 + s * 16 + 8 * half);
  if (ffn) {
#pragma unroll
    for (int i = 0; i < 4; ++i) w1r[i] = ld_nt16(w1 + (size_t)(row0 + i) * DFF + job * 16 + 8 * half);
  } else {
#pragma unroll
    for (int i = 0; i < 4; ++i) w1r[i] = u32x4{0u, 0u, 0u, 0u};
  }
  const unsigned xo = *(const GAS unsigned*)(x + 2 * c);
  __builtin_amdgcn_s_barrier();

  const unsigned long long tag = (((unsigned long long)ep[1] << 32) | ep[0]) + 1ull;
  const unsigned tlo = __builtin_amdgcn_readfirstlane((unsigned)tag), thi = __builtin_amdgcn_readfirstlane((unsigned)(tag >> 32));

  float xf[8];
#pragma unroll
  for (int k = 0; k < 4; ++k) { xf[2 * k] = bflo(xr[k]); xf[2 * k + 1] = bfhi(xr[k]); }
  float sum = 0.f;
#pragma unroll
  for (int k = 0; k < 8; ++k) sum += xf[k];
  const float mean = wave_sum(sum) * (1.f / DM);
  float sq = 0.f;
#pragma unroll
  for (int k = 0; k < 8; ++k) { const float d = xf[k] - mean; sq = fmaf(d, d, sq); }
  const float var = wave_sum(sq) * (1.f / DM);
  const float mb = rbf(mean), sd = rbf(sqrtf(rbf(rbf(var) + 1e-5f)));
  {
    f32x4 a, b;
#pragma unroll
    for (int k = 0; k < 4; ++k) { a[k] = rbf(rbf(xf[k] - mb) / sd); b[k] = rbf(rbf(xf[4 + k] - mb) / sd); }
    *(LAS f32x4*)(L + L_XN + 32 * lane) = a;
    *(LAS f32x4*)(L + L_XN + 32 * lane + 16) = b;
  }
  asm volatile("s_waitcnt lgkmcnt(0)" ::: "memory");
  const f32x4 xn4 = *(const LAS f32x4*)(L + L_XN + 4 * row0);

  LAS float* red = (LAS float*)(L + L_RED);
  {
    float acc[8];
    slice_dot(wqr, xn4, acc);
    if (lane < 2) {
#pragma unroll
      for (int k = 0; k < 8; ++k) red[cw * 16 + 8 * lane + k] = acc[k];
    }
    if (ffn) {
      slice_dot(w1r, xn4, acc);
      if (lane < 2) {
#pragma unroll
        for (int k = 0; k < 8; ++k) red[64 + cw * 16 + 8 * lane + k] = acc[k];
      }
    }
  }
  cons_sync(ctrl + 8, 4, lane);
  if (cw == 0 && lane < 4) {
    float q4[4];
#pragma unroll
    for (int k = 0; k < 4; ++k) { const int n = 4 * lane + k; q4[k] = (red[n] + red[16 + n]) + (red[32 + n] + red[48 + n]); }
    st_gran(ws, W_Q + (unsigned)(g * 128 + s * 4 + lane) * 16, pack2(q4[0], q4[1]), pack2(q4[2], q4[3]), tlo, thi);
  }
  unsigned wor[16], w2r[16];
#pragma unroll
  for (int r = 0; r < 16; ++r) wor[r] = ld_nt4(wo + (size_t)(16 * c + r) * DM + 2 * t);
  if (ffn) {
#pragma unroll
    for (int r = 0; r < 16; ++r) w2r[r] = ld_nt4(w2 + (size_t)(16 * job + r) * DM + 2 * t);
  }

  {
    u32x4 a, b;
    for (unsigned spins = 0; spins < SPIN_CAP; ++spins) {
      asm volatile("" ::: "memory");
      a = ld_gran(ws, W_Q + (unsigned)(g * 128 + lane) * 16);
      b = ld_gran(ws, W_Q + (unsigned)(g * 128 + 64 + lane) * 16);
      const bool ok = a[1] == tlo && a[3] == thi && b[1] == tlo && b[3] == thi;
      if (__all(ok)) break;
    }
    *(LAS u32x2*)(L + L_Q + 8 * lane) = u32x2{a[0], a[2]};
    *(LAS u32x2*)(L + L_Q + 512 + 8 * lane) = u32x2{b[0], b[2]};
    asm volatile("s_waitcnt lgkmcnt(0)" ::: "memory");
  }
  bf16x8 qf[4];
  {
    const int col = lane & 15, gg = lane >> 4;
#pragma unroll
    for (int m = 0; m < 4; ++m) qf[m] = *(const LAS bf16x8*)(L + L_Q + (col & 3) * 256 + 64 * m + 16 * gg);
  }

  if (ffn) {
    float b0 = 0.f, b1 = 0.f;
#pragma unroll
    for (int r = 0; r < 16; ++r) {
      const float h = fmaxf(rbf((red[64 + r] + red[80 + r]) + (red[96 + r] + red[112 + r])), 0.f);
      b0 = fmaf(h, bflo(w2r[r]), b0); b1 = fmaf(h, bfhi(w2r[r]), b1);
    }
    st_gran(ws, W_F + (unsigned)(((t >> 3) * NFFN + job) * 8 + (t & 7)) * 16, __float_as_uint(b0), __float_as_uint(b1), tlo, thi);
  }

  constexpr float CS = 0.08838834764831845f * 1.4426950408889634f;
  f32x4 acc[8], accl = {0.f, 0.f, 0.f, 0.f};
#pragma unroll
  for (int b = 0; b < 8; ++b) acc[b] = f32x4{0.f, 0.f, 0.f, 0.f};
  float mrun = -INFINITY, mc = 0.f;
  unsigned ka[4], va[8];
  {
    const unsigned rho = lane & 15, gg = lane >> 4;
    const unsigned ring0 = (unsigned)(size_t)(L + L_RING);
#pragma unroll
    for (int m = 0; m < 4; ++m) ka[m] = ring0 + rho * 256 + 16 * ((4 * m + gg) ^ rho);
    const unsigned q = (lane & 15) >> 2, p = lane & 3, row = 4 * gg + q;
#pragma unroll
    for (int b = 0; b < 8; ++b) va[b] = ring0 + 4096 + row * 256 + 8 * p + 32 * (b ^ (row & 7));
  }
  const s16x4 ones = {0x3F80, 0x3F80, 0x3F80, 0x3F80};
  for (int k = 0; k < NFILL / 4; ++k) {
    const unsigned j = 4 * k + cw;
    for (unsigned spins = 0; ctrl[0] <= j && spins < SPIN_CAP; ++spins) __builtin_amdgcn_s_sleep(1);
    asm volatile("" ::: "memory");
    const unsigned sb = (j & (RING - 1)) * SLOT;
    bf16x8 kf[4];
    bf16x4 vf[8];
#pragma unroll
    for (int m = 0; m < 4; ++m) kf[m] = *(const LAS bf16x8*)(ka[m] + sb);
#pragma unroll
    for (int b = 0; b < 8; ++b) vf[b] = __builtin_amdgcn_ds_read_tr16_b64_v4bf16((LAS bf16x4*)(va[b] + sb));
    f32x4 sc = {0.f, 0.f, 0.f, 0.f};
#pragma unroll
    for (int m = 0; m < 4; ++m) sc = __builtin_amdgcn_mfma_f32_16x16x32_bf16(kf[m], qf[m], sc, 0, 0, 0);
    const float mx = fmaxf(fmaxf(sc[0], sc[1]), fmaxf(sc[2], sc[3]));
    if (__any(mx > mrun)) {
      const float mn = fmaxf(mrun, xrow_max(mx));
      const float alpha = __builtin_amdgcn_exp2f((mrun - mn) * CS);
      mrun = mn; mc = mn * CS;
#pragma unroll
      for (int b = 0; b < 8; ++b) acc[b] *= alpha;
      accl *= alpha;
    }
    const float p0 = __builtin_amdgcn_exp2f(fmaf(sc[0], CS, -mc)), p1 = __builtin_amdgcn_exp2f(fmaf(sc[1], CS, -mc));
    const float p2 = __builtin_amdgcn_exp2f(fmaf(sc[2], CS, -mc)), p3 = __builtin_amdgcn_exp2f(fmaf(sc[3], CS, -mc));
    const u32x2 pp = {pack2(p0, p1), pack2(p2, p3)};
    const s16x4 pf = __builtin_bit_cast(s16x4, pp);
    asm volatile("s_waitcnt lgkmcnt(0)" ::: "memory");
    if (lane == 0) ctrl[4 + cw] = (unsigned)k + 1u;
    accl = __builtin_amdgcn_mfma_f32_16x16x16bf16_1k(ones, pf, accl, 0, 0, 0);
#pragma unroll
    for (int b = 0; b < 8; ++b) acc[b] = __builtin_amdgcn_mfma_f32_16x16x16bf16_1k(__builtin_bit_cast(s16x4, vf[b]), pf, acc[b], 0, 0, 0);
  }

  {
    const int head = lane & 3, rep = (lane >> 2) & 3, gg = lane >> 4;
    LAS float* mg = (LAS float*)(L + L_MRG) + (cw * 4 + head) * 128;
#pragma unroll
    for (int b = 0; b < 8; ++b) if ((b >> 1) == rep) *(LAS f32x4*)(mg + 16 * b + 4 * gg) = acc[b];
    if (lane < 4) { LAS float* ml = (LAS float*)(L + L_ML) + (cw * 4 + head) * 2; ml[0] = mc; ml[1] = accl[0]; }
  }
  __syncthreads();
  {
    const int head = cw;
    const LAS float* ml = (const LAS float*)(L + L_ML);
    float mw[4], M = -INFINITY;
#pragma unroll
    for (int v = 0; v < 4; ++v) { mw[v] = ml[(v * 4 + head) * 2]; M = fmaxf(M, mw[v]); }
    float Ls = 0.f, o0 = 0.f, o1 = 0.f;
#pragma unroll
    for (int v = 0; v < 4; ++v) {
      const float wt = __builtin_amdgcn_exp2f(mw[v] - M);
      Ls = fmaf(wt, ml[(v * 4 + head) * 2 + 1], Ls);
      const LAS float* mg = (const LAS float*)(L + L_MRG) + (v * 4 + head) * 128 + 2 * lane;
      o0 = fmaf(wt, mg[0], o0); o1 = fmaf(wt, mg[1], o1);
    }
    st_gran(ws, W_O + (unsigned)(((g * 4 + head) * 32 + s) * 64 + lane) * 16, __float_as_uint(o0), __float_as_uint(o1), tlo, thi);
    if (lane == 0) st_gran(ws, W_ML + (unsigned)((g * 4 + head) * 32 + s) * 16, __float_as_uint(M), __float_as_uint(Ls), tlo, thi);
  }

  float wof[32];
#pragma unroll
  for (int r = 0; r < 16; ++r) { wof[2 * r] = bflo(wor[r]); wof[2 * r + 1] = bfhi(wor[r]); }
  {
    const int hl = (c >> 3) & 3, dp = c & 7;
    const int sp = t >> 3, jj = t & 7;
    u32x4 og, mg;
    for (unsigned spins = 0; spins < SPIN_CAP; ++spins) {
      asm volatile("" ::: "memory");
      og = ld_gran(ws, W_O + (unsigned)(((g * 4 + hl) * 32 + sp) * 64 + 8 * dp + jj) * 16);
      mg = ld_gran(ws, W_ML + (unsigned)((g * 4 + hl) * 32 + (lane & 31)) * 16);
      const bool ok = og[1] == tlo && og[3] == thi && mg[1] == tlo && mg[3] == thi;
      if (__all(ok)) break;
    }
    const float mi = __uint_as_float(mg[0]), li = __uint_as_float(mg[2]);
    float M = mi;
    M = fmaxf(M, dpp<XOR1>(M)); M = fmaxf(M, dpp<XOR2>(M)); M = fmaxf(M, dpp<ROR4>(M)); M = fmaxf(M, dpp<ROR8>(M));
    M = xrow_max(M);
    const float wi = __builtin_amdgcn_exp2f(mi - M);
    const float Lsum = 0.5f * wave_sum(wi * li);
    const float wsel = __shfl(wi, sp & 31);
    float o0 = wsel * __uint_as_float(og[0]), o1 = wsel * __uint_as_float(og[2]);
    o0 += dpp<ROR8>(o0); o1 += dpp<ROR8>(o1);
    o0 = xrow_sum(o0); o1 = xrow_sum(o1);
    LAS float* r2 = (LAS float*)(L + L_RED);
    if (lane < 8) { r2[cw * 16 + 2 * lane] = o0; r2[cw * 16 + 2 * lane + 1] = o1; }
    __syncthreads();
    const int d = lane & 15;
    const float od = rbf(((r2[d] + r2[16 + d]) + (r2[32 + d] + r2[48 + d])) / Lsum);
    float s0 = 0.f, s1 = 0.f;
#pragma unroll
    for (int r = 0; r < 16; ++r) {
      const float o = __builtin_bit_cast(float, __builtin_amdgcn_readlane(__builtin_bit_cast(int, od), r));
      s0 = fmaf(o, wof[2 * r], s0); s1 = fmaf(o, wof[2 * r + 1], s1);
    }
    st_gran(ws, W_S + (unsigned)(((t >> 3) * 256 + c) * 8 + (t & 7)) * 16, __float_as_uint(s0), __float_as_uint(s1), tlo, thi);
  }

  {
    u32x4 sg, fg;
    const unsigned soff = W_S + (unsigned)(((c >> 3) * 256 + t) * 8 + (c & 7)) * 16;
    const unsigned foff = W_F + (unsigned)(((c >> 3) * NFFN + (t < NFFN ? t : 0)) * 8 + (c & 7)) * 16;
    for (unsigned spins = 0; spins < SPIN_CAP; ++spins) {
      asm volatile("" ::: "memory");
      sg = ld_gran(ws, soff);
      fg = ld_gran(ws, foff);
      const bool ok = sg[1] == tlo && sg[3] == thi && fg[1] == tlo && fg[3] == thi;
      if (__all(ok)) break;
    }
    float a0 = __uint_as_float(sg[0]), a1 = __uint_as_float(sg[2]);
    float b0 = t < NFFN ? __uint_as_float(fg[0]) : 0.f, b1 = t < NFFN ? __uint_as_float(fg[2]) : 0.f;
    a0 = wave_sum(a0); a1 = wave_sum(a1); b0 = wave_sum(b0); b1 = wave_sum(b1);
    LAS float* r3 = (LAS float*)(L + L_RED) + 128;
    if (lane == 0) { r3[cw * 4] = a0; r3[cw * 4 + 1] = a1; r3[cw * 4 + 2] = b0; r3[cw * 4 + 3] = b1; }
    __syncthreads();
    if (t == 0) {
      float o[2];
#pragma unroll
      for (int e = 0; e < 2; ++e) {
        const float sv = rbf((r3[e] + r3[4 + e]) + (r3[8 + e] + r3[12 + e]));
        const float bv = rbf((r3[2 + e] + r3[6 + e]) + (r3[10 + e] + r3[14 + e]));
        const float gv = rbf(bv / (1.f + __expf(-bv)));
        const float xv = e ? bfhi(xo) : bflo(xo);
        o[e] = xv + rbf(sv * gv);
      }
      *(GAS unsigned*)(out + 2 * c) = pack2(o[0], o[1]);
      if (w == 0) __builtin_amdgcn_raw_buffer_store_b64(u32x2{tlo, thi}, ws, W_EPOCH, 0, 16);
    }
  }
}

}

extern "C" void kernel_launch(void* const* d_in, const int* in_sizes, int n_in, void* d_out, int out_size, void* d_ws, size_t ws_size,
                              hipStream_t stream) {
  (void)in_sizes; (void)n_in; (void)out_size; (void)ws_size;
  (void)hipFuncSetAttribute((const void*)fused_decode_kernel, hipFuncAttributeMaxDynamicSharedMemorySize, L_END);
  hipLaunchKernelGGL(fused_decode_kernel, dim3(NWG), dim3(NTHR), L_END, stream,
                     (const unsigned short*)d_in[0], (const unsigned short*)d_in[1], (const unsigned short*)d_in[2],
                     (const unsigned short*)d_in[3], (const unsigned short*)d_in[4], (const unsigned short*)d_in[6],
                     (const unsigned short*)d_in[7], (unsigned short*)d_out, (unsigned char*)d_ws);
}
```

```cpp
#include <hip/hip_runtime.h>
#include <hip/hip_bf16.h>
#include <stdint.h>

#define LAS __attribute__((address_space(3)))
#define GAS __attribute__((address_space(1)))
typedef unsigned u32x4 __attribute__((ext_vector_type(4)));
typedef unsigned u32x2 __attribute__((ext_vector_type(2)));
typedef int i32x4 __attribute__((ext_vector_type(4)));
typedef float f32x4 __attribute__((ext_vector_type(4)));
typedef float f32x2 __attribute__((ext_vector_type(2)));
typedef __bf16 bf16x8 __attribute__((ext_vector_type(8)));
typedef __bf16 bf16x4 __attribute__((ext_vector_type(4)));
typedef __bf16 bf16x2 __attribute__((ext_vector_type(2)));
typedef short s16x4 __attribute__((ext_vector_type(4)));

namespace {

constexpr int DM = 512, HKV = 8, SK = 65536, DH = 128, DFF = 1792;
constexpr int NWG = 256, NTHR = 384;
#define FFN_SHED 14
#define RING_ 16
constexpr int RING = RING_, SLOT = 8192;
#define INFL_ 3
#define NBURST 3
#define AUX_FIRST 12
#define AUX_STEP 6
#define WAITN_ (8 * (INFL_ - 1))
constexpr int WAITN = WAITN_;
constexpr int INFL = (WAITN + 7) / 8 + 1;
constexpr int NJOB = DFF / 64;
constexpr int NFIN = 64;
constexpr unsigned KV_BYTES = (unsigned)HKV * SK * DH * 2;

constexpr int L_RING = 0;
constexpr int L_WO = RING * SLOT;
constexpr int L_CTRL = L_WO + 16384;
constexpr int L_XN = L_CTRL + 256;
constexpr int L_Q = L_XN + 2048;
constexpr int L_RED = L_Q + 1024;
constexpr int L_FT = L_RED + 1024;
constexpr int L_MRG = L_FT + 2048;
constexpr int L_ML = L_MRG + 8192;
constexpr int L_END = L_ML + 256;
static_assert(L_END <= 160 * 1024, "LDS");

constexpr unsigned W_EPOCH = 0;
constexpr unsigned W_Q = 4096;
constexpr unsigned W_O = W_Q + 8 * 8 * 4 * 32 * 16;
constexpr unsigned W_S = W_O + 8 * 4 * 8 * 32 * 8 * 16;
constexpr unsigned W_F = W_S + NFIN * 256 * 4 * 16;
constexpr unsigned W_END = W_F + NJOB * 256 * 16;

constexpr unsigned SPIN_CAP = 1u << 20;
#define LDSU(x) ((unsigned)__builtin_amdgcn_readfirstlane(x))
#define NPRE_ 3
constexpr int NPRE = NPRE_;
#define LDELAY 0
#define POLL_SLEEP 2

#define STAMP(i) do {} while (0)
#define STAMP_FLUSH() do {} while (0)

__device__ __forceinline__ float bf2f(unsigned short b) { return __builtin_bit_cast(float, (unsigned)b << 16); }
__device__ __forceinline__ float bflo(unsigned u) { return __builtin_bit_cast(float, u << 16); }
__device__ __forceinline__ float bfhi(unsigned u) { return __builtin_bit_cast(float, u & 0xffff0000u); }
__device__ __forceinline__ unsigned short f2bf(float f) { return __builtin_bit_cast(unsigned short, (__bf16)f); }
__device__ __forceinline__ float rbf(float f) { return bf2f(f2bf(f)); }
__device__ __forceinline__ unsigned pack2(float lo, float hi) {
  const f32x2 v = {lo, hi};
  return __builtin_bit_cast(unsigned, __builtin_convertvector(v, bf16x2));
}

template <int CTRL> __device__ __forceinline__ float dpp(float x) {
  return __builtin_bit_cast(float, __builtin_amdgcn_mov_dpp(__builtin_bit_cast(int, x), CTRL, 0xf, 0xf, true));
}
template <int CTRL> __device__ __forceinline__ unsigned dppu(unsigned x) {
  return (unsigned)__builtin_amdgcn_mov_dpp((int)x, CTRL, 0xf, 0xf, true);
}
constexpr int XOR1 = 0xB1, XOR2 = 0x4E, ROR4 = 0x124, ROR8 = 0x128;
__device__ __forceinline__ float xrow_sum(float x) {
  auto s = __builtin_amdgcn_permlane16_swap(__float_as_uint(x), __float_as_uint(x), false, false);
  x = __uint_as_float(s[0]) + __uint_as_float(s[1]);
  auto t = __builtin_amdgcn_permlane32_swap(__float_as_uint(x), __float_as_uint(x), false, false);
  return __uint_as_float(t[0]) + __uint_as_float(t[1]);
}
__device__ __forceinline__ float xrow_max(float x) {
  auto s = __builtin_amdgcn_permlane16_swap(__float_as_uint(x), __float_as_uint(x), false, false);
  x = fmaxf(__uint_as_float(s[0]), __uint_as_float(s[1]));
  auto t = __builtin_amdgcn_permlane32_swap(__float_as_uint(x), __float_as_uint(x), false, false);
  return fmaxf(__uint_as_float(t[0]), __uint_as_float(t[1]));
}
__device__ __forceinline__ float wave_sum(float x) {
  x += dpp<XOR1>(x); x += dpp<XOR2>(x); x += dpp<ROR4>(x); x += dpp<ROR8>(x);
  return xrow_sum(x);
}
__device__ __forceinline__ float sum_mod2(float x) {
  x += dpp<XOR2>(x); x += dpp<ROR4>(x); x += dpp<ROR8>(x);
  return xrow_sum(x);
}
__device__ __forceinline__ float sum_mod4(float x) {
  x += dpp<ROR4>(x); x += dpp<ROR8>(x);
  return xrow_sum(x);
}
__device__ __forceinline__ float sum_mod8(float x) { x += dpp<ROR8>(x); return xrow_sum(x); }
__device__ __forceinline__ float max_mod8(float x) { x = fmaxf(x, dpp<ROR8>(x)); return xrow_max(x); }

__device__ __forceinline__ u32x4 ld_nt16(const void* p) { return __builtin_nontemporal_load((const GAS u32x4*)p); }

__device__ __forceinline__ void st_gran(__amdgpu_buffer_rsrc_t ws, unsigned off, unsigned a, unsigned b, unsigned c, unsigned d) {
  const u32x4 g = {a, b, c, d};
  __builtin_amdgcn_raw_buffer_store_b128(g, ws, off, 0, 16);
}
__device__ __forceinline__ u32x4 ld_gran(__amdgpu_buffer_rsrc_t ws, unsigned off) {
  return __builtin_amdgcn_raw_buffer_load_b128(ws, off, 0, 16);
}

__device__ __forceinline__ int job_of(int g, int s) { return g * 4 + (s - 8); }

__global__ void __launch_bounds__(NTHR, 2)
fused_decode_kernel(const unsigned short* __restrict__ x, const unsigned short* __restrict__ kc, const unsigned short* __restrict__ vc,
                    const unsigned short* __restrict__ wq, const unsigned short* __restrict__ wo, const unsigned short* __restrict__ w1,
                    const unsigned short* __restrict__ w2, unsigned short* __restrict__ out, unsigned char* __restrict__ wsp) {
  extern __shared__ __attribute__((aligned(16))) unsigned char lds[];
  LAS unsigned char* L = (LAS unsigned char*)lds;
  volatile LAS unsigned* ctrl = (volatile LAS unsigned*)(L + L_CTRL);
  const int w = blockIdx.x, g = w & 7, s = w >> 3;
  const int wave = __builtin_amdgcn_readfirstlane(threadIdx.x >> 6), lane = threadIdx.x & 63;
  constexpr int NF_J = 128 - FFN_SHED, NF_O = 128 + FFN_SHED / 7;
  const bool has_job = g < 7 && s >= 8 && s < 12;
  const int nfill = g == 7 ? 128 : has_job ? NF_J : NF_O;
  const int fill0 = g == 7 ? 128 * s : s <= 8 ? NF_O * s : s <= 12 ? NF_O * 8 + NF_J * (s - 8) : NF_O * (s - 4) + NF_J * 4;
  STAMP(0);

  if (wave == 0) {
    const unsigned long long kb = (unsigned long long)kc, vb = (unsigned long long)vc;
    const i32x4 sk = {(int)kb, (int)(kb >> 32), (int)KV_BYTES, 0x00020000};
    const i32x4 sv = {(int)vb, (int)(vb >> 32), (int)KV_BYTES, 0x00020000};
    const unsigned base = (unsigned)g * (SK * DH * 2) + (unsigned)fill0 * 4096;
    const unsigned r4 = lane >> 4, p = lane & 15;
    const unsigned k0 = r4 * 256 + 16 * (p ^ (0 + r4)), k1 = r4 * 256 + 16 * (p ^ (4 + r4));
    const unsigned k2 = r4 * 256 + 16 * (p ^ (8 + r4)), k3 = r4 * 256 + 16 * (p ^ (12 + r4));
    const unsigned v0 = r4 * 256 + 16 * (p ^ (r4 << 1)), v1 = r4 * 256 + 16 * (p ^ ((4 + r4) << 1));
    const unsigned ring0 = (unsigned)(size_t)(L + L_RING);
    __builtin_amdgcn_s_setprio(3);
    const int naux = has_job ? 20 : 4, job = g * 4 + (s - 8);
    const unsigned vq = (lane >> 3) * 8192 + (lane & 7) * 16, v1w = (lane >> 3) * (DFF * 2) + (lane & 7) * 16, vc16 = lane * 16;
    const unsigned aux0 = (unsigned)(size_t)(L + L_WO);
    const unsigned long long qb = (unsigned long long)wq, ob = (unsigned long long)wo, b1 = (unsigned long long)w1, b2 = (unsigned long long)w2;
    const i32x4 sq = {(int)qb, (int)(qb >> 32), DM * 4096 * 2, 0x00020000}, so = {(int)ob, (int)(ob >> 32), 4096 * DM * 2, 0x00020000};
    const i32x4 s1 = {(int)b1, (int)(b1 >> 32), DM * DFF * 2, 0x00020000}, s2 = {(int)b2, (int)(b2 >> 32), DM * DFF * 2, 0x00020000};
    if (lane < 16) ctrl[lane] = 0u;
    asm volatile("s_waitcnt lgkmcnt(0)" ::: "memory");
    __builtin_amdgcn_s_barrier();
    if (LDELAY) __builtin_amdgcn_s_sleep(LDELAY);
#define PARK_BEGIN
#define PARK_END
#define PROGRESS
#define KV_ISSUE(WAIT) asm volatile( \
            WAIT "s_mov_b32 m0, %0\n\ts_nop 0\n\t" \
            "buffer_load_dwordx4 %2, %8, %10 offen nt lds\n\t" \
            "buffer_load_dwordx4 %3, %8, %10 offen offset:1024 nt lds\n\t" \
            "buffer_load_dwordx4 %4, %8, %10 offen offset:2048 nt lds\n\t" \
            "buffer_load_dwordx4 %5, %8, %10 offen offset:3072 nt lds\n\t" \
            "s_mov_b32 m0, %1\n\ts_nop 0\n\t" \
            "buffer_load_dwordx4 %6, %9, %10 offen nt lds\n\t" \
            "buffer_load_dwordx4 %7, %9, %10 offen offset:1024 nt lds\n\t" \
            "buffer_load_dwordx4 %6, %9, %10 offen offset:2048 nt lds\n\t" \
            "buffer_load_dwordx4 %7, %9, %10 offen offset:3072 nt lds" \
            :: "s"(m0k), "s"(m0k + 4096), "v"(k0), "v"(k1), "v"(k2), "v"(k3), "v"(v0), "v"(v1), "s"(sk), "s"(sv), "s"(sko), \
               "i"(WAITN) : "memory")
#define SIDE_ISSUE(SRD, VO, SO0, RSTEP) do { \
          const unsigned so0_ = (SO0), st_ = (RSTEP) - 1024, so4_ = so0_ + 4 * (RSTEP); \
          asm volatile( \
            "s_waitcnt vmcnt(%12)\n\t" \
            "s_mov_b32 m0, %0\n\ts_nop 0\n\t" \
            "buffer_load_dwordx4 %2, %3, %4 offen nt lds\n\t" \
            "buffer_load_dwordx4 %2, %3, %5 offen offset:1024 nt lds\n\t" \
            "buffer_load_dwordx4 %2, %3, %6 offen offset:2048 nt lds\n\t" \
            "buffer_load_dwordx4 %2, %3, %7 offen offset:3072 nt lds\n\t" \
            "s_mov_b32 m0, %1\n\ts_nop 0\n\t" \
            "buffer_load_dwordx4 %2, %3, %8 offen nt lds\n\t" \
            "buffer_load_dwordx4 %2, %3, %9 offen offset:1024 nt lds\n\t" \
            "buffer_load_dwordx4 %2, %3, %10 offen offset:2048 nt lds\n\t" \
            "buffer_load_dwordx4 %2, %3, %11 offen offset:3072 nt lds" \
            :: "s"(dst), "s"(dst + 4096), "v"(VO), "s"(SRD), "s"(so0_), "s"(so0_ + st_), "s"(so0_ + 2 * st_), "s"(so0_ + 3 * st_), \
               "s"(so4_), "s"(so4_ + st_), "s"(so4_ + 2 * st_), "s"(so4_ + 3 * st_), "i"(WAITN) : "memory"); } while (0)
    auto side_pos = [&](int a) -> int { return a >= naux ? 0x7fffffff : a < 2 ? a : has_job ? AUX_FIRST + AUX_STEP * (a - 2) : 24 + 6 * (a - 2); };
    unsigned m0k = ring0, sko = base;
#pragma unroll
    for (int a = 0; a < 2; ++a) {
      const unsigned dst = aux0 + (unsigned)a * 8192;
      SIDE_ISSUE(sq, vq, (unsigned)(128 * (s >> 3) + 64 * a) * 8192 + g * 1024 + (s & 7) * 128, 8 * 8192u);
    }
#pragma unroll
    for (int j = 0; j < NBURST - 2; ++j) { KV_ISSUE(""); m0k += SLOT; sko += 4096; }
    asm volatile("s_waitcnt vmcnt(%0)" :: "i"(8 * (NBURST - 2)) : "memory");
    *(volatile LAS u32x2*)(L + L_CTRL) = u32x2{0u, 2u};
    int kv = NBURST - 2, ax = 2, e = NBURST, le = 2, axl = 2, lim = RING, alim = 2;
    int nextpos = side_pos(2), nextland = nextpos;
    const int nent = nfill + naux;
    const unsigned ring_end = ring0 + RING * SLOT;
    u32x4 dn = {0u, 0u, 0u, 0u};
    unsigned da = 0u;
#define ACCOUNT() do { \
        le = max(le, e + 1 - INFL); \
        if (le > nextland) { ++axl; nextland = side_pos(axl); } \
        *(volatile LAS u32x2*)(L + L_CTRL) = u32x2{(unsigned)(le - axl), (unsigned)axl}; \
        lim = max(lim, RING + 4 * (int)LDSU(min(min(dn[0], dn[1]), min(dn[2], dn[3])))); \
        alim = max(alim, 2 + (int)LDSU(da)); \
        dn = *(volatile LAS u32x4*)(L + L_CTRL + 16); \
        da = ctrl[3]; \
      } while (0)
#define PARK(COND) do { \
        asm volatile("s_waitcnt vmcnt(0)" ::: "memory"); \
        le = e; while (le > nextland) { ++axl; nextland = side_pos(axl); } \
        *(volatile LAS u32x2*)(L + L_CTRL) = u32x2{(unsigned)(le - axl), (unsigned)axl}; \
        PARK_BEGIN \
        for (unsigned spins = 0; spins < SPIN_CAP; ++spins) { if (COND) break; __builtin_amdgcn_s_sleep(1); } \
        PARK_END \
        asm volatile("" ::: "memory"); \
      } while (0)
    while (e < nent) {
      const int stop = min(nextpos, nent);
      for (; e < stop; ++e) {
        if (kv >= lim) { PARK(LDSU(ctrl[4 + ((kv - RING) & 3)]) > (unsigned)((kv - RING) >> 2)); lim = kv + 1; }
        KV_ISSUE("s_waitcnt vmcnt(%11)\n\t");
        m0k += SLOT; if (m0k == ring_end) m0k = ring0;
        sko += 4096;
        ++kv;
        ACCOUNT();
        PROGRESS
      }
      if (e < nent) {
        if (ax >= alim) { PARK(LDSU(ctrl[3]) + 2u > (unsigned)ax); alim = ax + 1; }
        const unsigned dst = aux0 + (unsigned)(ax & 1) * 8192;
        if (ax >= naux - 2) SIDE_ISSUE(so, vc16, (unsigned)(16 * (g * 32 + s) + 8 * (ax - (naux - 2))) * 1024, 1024u);
        else if (ax < 10) SIDE_ISSUE(s1, v1w, (unsigned)(64 * (ax - 2)) * (DFF * 2) + job * 128, 8u * DFF * 2);
        else SIDE_ISSUE(s2, vc16, (unsigned)(64 * job + 8 * (ax - 10)) * 1024, 1024u);
        ++ax;
        nextpos = side_pos(ax);
        ACCOUNT();
        ++e;
      }
    }
#pragma unroll
    for (int r = INFL - 1; r >= 0; --r) {
      asm volatile("s_waitcnt vmcnt(%0)" :: "i"(8 * r) : "memory");
      le = max(le, nent - r);
      while (le > nextland) { ++axl; nextland = side_pos(axl); }
      *(volatile LAS u32x2*)(L + L_CTRL) = u32x2{(unsigned)(le - axl), (unsigned)axl};
    }
#undef KV_ISSUE
#undef SIDE_ISSUE
#undef ACCOUNT
#undef PARK
    STAMP(2);
    STAMP_FLUSH();
    return;
  }

  const __amdgpu_buffer_rsrc_t ws = __builtin_amdgcn_make_buffer_rsrc((void*)wsp, 0, (int)W_END, 0x00020000);
  const int c = g * 32 + s;
  const int fin = g * 8 + s;

  if (wave == 5) {
    const u32x2 ep = __builtin_amdgcn_raw_buffer_load_b64(ws, W_EPOCH, 0, 16);
    unsigned touch;
    {
      const unsigned char* p = (const unsigned char*)x;
      if (lane == 1) p = (const unsigned char*)wo + (size_t)c * 16384;
      if (lane == 2) p = (const unsigned char*)w1 + (size_t)(has_job ? job_of(g, s) * 128 : 0);
      if (lane == 3) p = (const unsigned char*)w1 + (size_t)(DM - 1) * DFF * 2;
      if (lane == 4) p = (const unsigned char*)w2 + (size_t)(has_job ? job_of(g, s) * 65536 : 0);
      if (lane == 5) p = wsp + W_O + (size_t)g * 131072;
      if (lane == 6) p = wsp + W_S;
      if (lane == 7) p = wsp + W_END - 4;
      if (lane == 8) p = (const unsigned char*)out;
      touch = *(const GAS unsigned*)p;
    }
    __builtin_amdgcn_s_barrier();
    asm volatile("" :: "v"(touch));
    for (unsigned spins = 0; LDSU(ctrl[8]) < 4u && spins < SPIN_CAP; ++spins) __builtin_amdgcn_s_sleep(4);
    asm volatile("" ::: "memory");
    const unsigned long long tag = (((unsigned long long)ep[1] << 32) | ep[0]) + 1ull;
    const unsigned tlo = __builtin_amdgcn_readfirstlane((unsigned)tag), thi = __builtin_amdgcn_readfirstlane((unsigned)(tag >> 32));
    if (has_job) {
      const int job = g * 4 + (s - 8);
      const int r8 = lane >> 3, cq = lane & 7;
      float h[8];
#pragma unroll
      for (int k = 0; k < 8; ++k) h[k] = 0.f;
#pragma unroll 1
      for (int p = 0; p < 8; ++p) {
        for (unsigned spins = 0; LDSU(ctrl[1]) < (unsigned)(3 + p) && spins < SPIN_CAP; ++spins) __builtin_amdgcn_s_sleep(4);
        asm volatile("" ::: "memory");
        const LAS unsigned char* src = L + L_WO + (p & 1) * 8192 + r8 * 128 + cq * 16;
#pragma unroll
        for (int i = 0; i < 8; ++i) {
          const u32x4 wr = *(const LAS u32x4*)(src + i * 1024);
          const float xn = *(const LAS float*)(L + L_XN + 4 * (64 * p + 8 * i + r8));
#pragma unroll
          for (int k = 0; k < 4; ++k) { h[2 * k] = fmaf(xn, bflo(wr[k]), h[2 * k]); h[2 * k + 1] = fmaf(xn, bfhi(wr[k]), h[2 * k + 1]); }
        }
        asm volatile("s_waitcnt lgkmcnt(0)" ::: "memory");
        if (lane == 0) ctrl[3] = (unsigned)(3 + p);
      }
#pragma unroll
      for (int k = 0; k < 8; ++k) h[k] = fmaxf(rbf(sum_mod8(h[k])), 0.f);
      float bp[8];
#pragma unroll
      for (int k = 0; k < 8; ++k) bp[k] = 0.f;
#pragma unroll 1
      for (int p = 0; p < 8; ++p) {
        for (unsigned spins = 0; LDSU(ctrl[1]) < (unsigned)(11 + p) && spins < SPIN_CAP; ++spins) __builtin_amdgcn_s_sleep(4);
        asm volatile("" ::: "memory");
        const LAS unsigned char* src = L + L_WO + (p & 1) * 8192 + lane * 16;
#pragma unroll
        for (int i = 0; i < 8; ++i) {
          const u32x4 wr = *(const LAS u32x4*)(src + i * 1024);
          const float hv = __builtin_bit_cast(float, __builtin_amdgcn_readlane(__builtin_bit_cast(int, h[i]), p));
#pragma unroll
          for (int k = 0; k < 4; ++k) { bp[2 * k] = fmaf(hv, bflo(wr[k]), bp[2 * k]); bp[2 * k + 1] = fmaf(hv, bfhi(wr[k]), bp[2 * k + 1]); }
        }
        asm volatile("s_waitcnt lgkmcnt(0)" ::: "memory");
        if (lane == 0) ctrl[3] = (unsigned)(11 + p);
      }
      *(LAS f32x4*)(L + L_FT + 32 * lane) = f32x4{bp[0], bp[1], bp[2], bp[3]};
      *(LAS f32x4*)(L + L_FT + 32 * lane + 16) = f32x4{bp[4], bp[5], bp[6], bp[7]};
      asm volatile("s_waitcnt lgkmcnt(0)" ::: "memory");
#pragma unroll
      for (int e = 0; e < 4; ++e) {
        const f32x2 v = *(const LAS f32x2*)(L + L_FT + 8 * (64 * e + lane));
        st_gran(ws, W_F + (unsigned)(job * 256 + 64 * e + lane) * 16, __float_as_uint(v[0]), tlo, __float_as_uint(v[1]), thi);
      }
    }
    if (s < 8) {
      u32x4 a, b;
      const unsigned oa = W_F + (unsigned)((lane >> 2) * 256 + fin * 4 + (lane & 3)) * 16;
      const bool two = lane < 4 * (NJOB - 16);
      for (unsigned spins = 0; spins < SPIN_CAP; ++spins) {
        asm volatile("" ::: "memory");
        a = ld_gran(ws, oa); b = ld_gran(ws, two ? oa + 16 * 256 * 16 : oa);
        if (__all(a[1] == tlo && a[3] == thi && b[1] == tlo && b[3] == thi)) break;
        __builtin_amdgcn_s_sleep(64);
      }
      const float b0 = rbf(sum_mod4(__uint_as_float(a[0]) + (two ? __uint_as_float(b[0]) : 0.f)));
      const float b1 = rbf(sum_mod4(__uint_as_float(a[2]) + (two ? __uint_as_float(b[2]) : 0.f)));
      const float g0 = rbf(b0 / (1.f + __expf(-b0))), g1 = rbf(b1 / (1.f + __expf(-b1)));
      if (lane < 4) *(LAS f32x2*)(L + L_RED + 768 + 8 * lane) = f32x2{g0, g1};
    }
    asm volatile("s_waitcnt lgkmcnt(0)" ::: "memory");
    if (lane == 0) ctrl[10] = 1u;
    return;
  }

  const int cw = wave - 1, t = threadIdx.x - 64;

  const u32x2 ep = __builtin_amdgcn_raw_buffer_load_b64(ws, W_EPOCH, 0, 16);
  const u32x4 xr = *(const GAS u32x4*)(x + 8 * lane);
  const int trow = 32 * cw + (lane >> 3);
  const unsigned xo = *(const GAS unsigned*)(x + 8 * (fin & (NFIN - 1)) + 2 * (lane & 3));
  __builtin_amdgcn_s_barrier();

  const unsigned long long tag = (((unsigned long long)ep[1] << 32) | ep[0]) + 1ull;
  const unsigned tlo = __builtin_amdgcn_readfirstlane((unsigned)tag), thi = __builtin_amdgcn_readfirstlane((unsigned)(tag >> 32));

  {
    float xf[8];
#pragma unroll
    for (int k = 0; k < 4; ++k) { xf[2 * k] = bflo(xr[k]); xf[2 * k + 1] = bfhi(xr[k]); }
    float sum = ((xf[0] + xf[1]) + (xf[2] + xf[3])) + ((xf[4] + xf[5]) + (xf[6] + xf[7]));
    const float mean = wave_sum(sum) * (1.f / DM);
    float sq = 0.f;
#pragma unroll
    for (int k = 0; k < 8; ++k) { const float d = xf[k] - mean; sq = fmaf(d, d, sq); }
    const float var = wave_sum(sq) * (1.f / DM);
    const float mb = rbf(mean), sd = rbf(__builtin_amdgcn_sqrtf(rbf(rbf(var) + 1e-5f)));
    const float rs = __builtin_amdgcn_rcpf(sd);
    f32x4 a, b;
#pragma unroll
    for (int k = 0; k < 4; ++k) { a[k] = rbf(rbf(xf[k] - mb) * rs); b[k] = rbf(rbf(xf[4 + k] - mb) * rs); }
    *(LAS f32x4*)(L + L_XN + 32 * lane) = a;
    *(LAS f32x4*)(L + L_XN + 32 * lane + 16) = b;
  }
  asm volatile("s_waitcnt lgkmcnt(0)" ::: "memory");
  float xq[4];
#pragma unroll
  for (int i = 0; i < 4; ++i) xq[i] = *(const LAS float*)(L + L_XN + 4 * (128 * (s >> 3) + trow + 8 * i));
  STAMP(1);
  for (unsigned spins = 0; LDSU(ctrl[1]) < 2u && spins < SPIN_CAP; ++spins) {}
  asm volatile("" ::: "memory");
  u32x4 wqr[4];
#pragma unroll
  for (int i = 0; i < 4; ++i) wqr[i] = *(const LAS u32x4*)(L + L_WO + (trow + 8 * i) * 128 + (lane & 7) * 16);

  LAS float* red = (LAS float*)(L + L_RED);
  {
    float acc[8];
#pragma unroll
    for (int k = 0; k < 8; ++k) acc[k] = 0.f;
#pragma unroll
    for (int i = 0; i < 4; ++i)
#pragma unroll
      for (int k = 0; k < 4; ++k) {
        acc[2 * k] = fmaf(xq[i], bflo(wqr[i][k]), acc[2 * k]);
        acc[2 * k + 1] = fmaf(xq[i], bfhi(wqr[i][k]), acc[2 * k + 1]);
      }
#pragma unroll
    for (int k = 0; k < 8; ++k) acc[k] = sum_mod8(acc[k]);
    if (lane < 8) {
      *(LAS f32x4*)(red + cw * 64 + 8 * lane) = f32x4{acc[0], acc[1], acc[2], acc[3]};
      *(LAS f32x4*)(red + cw * 64 + 8 * lane + 4) = f32x4{acc[4], acc[5], acc[6], acc[7]};
    }
  }
  asm volatile("s_waitcnt lgkmcnt(0)" ::: "memory");
  if (lane == 0) __hip_atomic_fetch_add((LAS unsigned*)(ctrl + 8), 1u, __ATOMIC_RELAXED, __HIP_MEMORY_SCOPE_WORKGROUP);
  if (cw == 0) {
    for (unsigned spins = 0; LDSU(ctrl[8]) < 4u && spins < SPIN_CAP; ++spins) {}
    asm volatile("" ::: "memory");
    if (lane == 0) ctrl[3] = 2u;
    if (lane < 32) {
      const f32x2 q2 = (*(const LAS f32x2*)(red + 2 * lane) + *(const LAS f32x2*)(red + 64 + 2 * lane)) +
                       (*(const LAS f32x2*)(red + 128 + 2 * lane) + *(const LAS f32x2*)(red + 192 + 2 * lane));
      st_gran(ws, W_Q + (unsigned)(((g * 8 + (s & 7)) * 4 + (s >> 3)) * 32 + lane) * 16, __float_as_uint(q2[0]), tlo, __float_as_uint(q2[1]), thi);
    }
  }
  STAMP(2);
  unsigned ka[4], va[8];
  {
    const unsigned rho = lane & 15, gg = lane >> 4;
    const unsigned ring0 = (unsigned)(size_t)(L + L_RING);
#pragma unroll
    for (int m = 0; m < 4; ++m) ka[m] = ring0 + rho * 256 + 16 * ((4 * m + gg) ^ rho);
    const unsigned q = (lane & 15) >> 2, p = lane & 3, row = 4 * gg + q;
#pragma unroll
    for (int b = 0; b < 8; ++b) va[b] = ring0 + 4096 + row * 256 + 8 * p + 32 * (b ^ (row & 7));
  }
  int slot = cw;
  auto fetch = [&](int k, bf16x8 (&kf)[4], bf16x4 (&vf)[8]) {
    const unsigned j = 4 * k + cw;
    for (unsigned spins = 0; LDSU(ctrl[0]) <= j && spins < SPIN_CAP; ++spins) __builtin_amdgcn_s_sleep(1);
    asm volatile("" ::: "memory");
    const unsigned sb = (unsigned)slot * SLOT;
    slot += 4; if (slot >= RING) slot -= RING;
#pragma unroll
    for (int m = 0; m < 4; ++m) kf[m] = *(const LAS bf16x8*)(size_t)(ka[m] + sb);
#pragma unroll
    for (int b = 0; b < 8; ++b) vf[b] = __builtin_amdgcn_ds_read_tr16_b64_v4bf16((LAS bf16x4*)(size_t)(va[b] + sb));
  };
  auto release = [&](int k) {
    asm volatile("s_waitcnt lgkmcnt(0)" ::: "memory");
    if (lane == 0) ctrl[4 + cw] = (unsigned)k + 1u;
  };
  bf16x8 kp[NPRE][4];
  bf16x4 vp[NPRE][8];
#pragma unroll
  for (int k = 0; k < NPRE; ++k) { fetch(k, kp[k], vp[k]); release(k); }
#pragma unroll
  for (int k = 0; k < NPRE; ++k) {
#pragma unroll
    for (int m = 0; m < 4; ++m) asm volatile("" : "+v"(kp[k][m]));
#pragma unroll
    for (int b = 0; b < 8; ++b) asm volatile("" : "+v"(vp[k][b]));
  }

  {
    u32x4 qa[4];
    const unsigned off = W_Q + (unsigned)((g * 8 + (t >> 5)) * 128 + (t & 31)) * 16;
    bool ok[4] = {false, false, false, false};
    for (unsigned spins = 0; spins < SPIN_CAP; ++spins) {
      asm volatile("" ::: "memory");
#pragma unroll
      for (int i = 0; i < 4; ++i) if (!ok[i]) qa[i] = ld_gran(ws, off + i * 512);
#pragma unroll
      for (int i = 0; i < 4; ++i) ok[i] = qa[i][1] == tlo && qa[i][3] == thi;
      if (__all(ok[0] && ok[1] && ok[2] && ok[3])) break;
      __builtin_amdgcn_s_sleep(POLL_SLEEP);
    }
    const float q0 = (__uint_as_float(qa[0][0]) + __uint_as_float(qa[1][0])) + (__uint_as_float(qa[2][0]) + __uint_as_float(qa[3][0]));
    const float q1 = (__uint_as_float(qa[0][2]) + __uint_as_float(qa[1][2])) + (__uint_as_float(qa[2][2]) + __uint_as_float(qa[3][2]));
    *(LAS unsigned*)(L + L_Q + 4 * t) = pack2(q0, q1);
    asm volatile("s_waitcnt lgkmcnt(0)" ::: "memory");
    if (lane == 0) __hip_atomic_fetch_add((LAS unsigned*)(ctrl + 9), 1u, __ATOMIC_RELAXED, __HIP_MEMORY_SCOPE_WORKGROUP);
    for (unsigned spins = 0; LDSU(ctrl[9]) < 4u && spins < SPIN_CAP; ++spins) {}
  }
  asm volatile("" ::: "memory");
  STAMP(3);
  bf16x8 qf[4];
  {
    const int col = lane & 15, gg = lane >> 4;
#pragma unroll
    for (int m = 0; m < 4; ++m) qf[m] = *(const LAS bf16x8*)(L + L_Q + (col & 3) * 256 + 64 * m + 16 * gg);
  }

  constexpr float CS = 0.08838834764831845f * 1.4426950408889634f;
  f32x4 acc[8], accl = {0.f, 0.f, 0.f, 0.f};
#pragma unroll
  for (int b = 0; b < 8; ++b) acc[b] = f32x4{0.f, 0.f, 0.f, 0.f};
  float mrun = -INFINITY, mc = 0.f;
  const s16x4 ones = {0x3F80, 0x3F80, 0x3F80, 0x3F80};
  auto scores = [&](const bf16x8 (&kf)[4]) -> s16x4 {
    f32x4 sc = {0.f, 0.f, 0.f, 0.f};
#pragma unroll
    for (int m = 0; m < 4; ++m) sc = __builtin_amdgcn_mfma_f32_16x16x32_bf16(kf[m], qf[m], sc, 0, 0, 0);
    const float mx = fmaxf(fmaxf(sc[0], sc[1]), fmaxf(sc[2], sc[3]));
    if (__any(mx > mrun)) {
      const float mn = fmaxf(mrun, xrow_max(mx));
      const float alpha = __builtin_amdgcn_exp2f((mrun - mn) * CS);
      mrun = mn; mc = mn * CS;
#pragma unroll
      for (int b = 0; b < 8; ++b) acc[b] *= alpha;
      accl *= alpha;
    }
    const float p0 = __builtin_amdgcn_exp2f(fmaf(sc[0], CS, -mc)), p1 = __builtin_amdgcn_exp2f(fmaf(sc[1], CS, -mc));
    const float p2 = __builtin_amdgcn_exp2f(fmaf(sc[2], CS, -mc)), p3 = __builtin_amdgcn_exp2f(fmaf(sc[3], CS, -mc));
    const u32x2 pp = {pack2(p0, p1), pack2(p2, p3)};
    return __builtin_bit_cast(s16x4, pp);
  };
  auto pv = [&](const bf16x4 (&vf)[8], s16x4 pf) {
    accl = __builtin_amdgcn_mfma_f32_16x16x16bf16_1k(ones, pf, accl, 0, 0, 0);
#pragma unroll
    for (int b = 0; b < 8; ++b) acc[b] = __builtin_amdgcn_mfma_f32_16x16x16bf16_1k(__builtin_bit_cast(s16x4, vf[b]), pf, acc[b], 0, 0, 0);
  };
  STAMP(4);
#pragma unroll
  for (int k = 0; k < NPRE; ++k) pv(vp[k], scores(kp[k]));
  for (int k = NPRE; 4 * k + cw < nfill; ++k) {
    bf16x8 kf[4];
    bf16x4 vf[8];
    fetch(k, kf, vf);
    const s16x4 pf = scores(kf);
    release(k);
    pv(vf, pf);
  }
  STAMP(5);

  {
    const int head = lane & 3, rep = (lane >> 2) & 3, gg = lane >> 4;
    LAS float* mg = (LAS float*)(L + L_MRG) + (cw * 4 + head) * 128 + 32 * rep + 4 * gg;
    const f32x4 va0 = rep == 0 ? acc[0] : rep == 1 ? acc[2] : rep == 2 ? acc[4] : acc[6];
    const f32x4 va1 = rep == 0 ? acc[1] : rep == 1 ? acc[3] : rep == 2 ? acc[5] : acc[7];
    *(LAS f32x4*)(mg) = va0;
    *(LAS f32x4*)(mg + 16) = va1;
    if (lane < 4) *(LAS f32x2*)(L + L_ML + (head * 4 + cw) * 8) = f32x2{mc, accl[0]};
  }
  for (unsigned spins = 0; (LDSU(ctrl[10]) == 0u || LDSU(ctrl[1]) < (has_job ? 20u : 4u)) && spins < SPIN_CAP; ++spins) __builtin_amdgcn_s_sleep(1);
  asm volatile("" ::: "memory");
  float wof[32];
#pragma unroll
  for (int r = 0; r < 16; ++r) {
    const unsigned u = *(const LAS unsigned*)(L + L_WO + r * 1024 + 4 * t);
    wof[2 * r] = bflo(u); wof[2 * r + 1] = bfhi(u);
  }
  asm volatile("s_waitcnt lgkmcnt(0)" ::: "memory");
  __builtin_amdgcn_s_barrier();
  const unsigned tb = thi & 0xffff0000u;
  {
    const int head = cw;
    const f32x4 m01 = *(const LAS f32x4*)(L + L_ML + head * 32), m23 = *(const LAS f32x4*)(L + L_ML + head * 32 + 16);
    const float M = fmaxf(fmaxf(m01[0], m01[2]), fmaxf(m23[0], m23[2]));
    const float f0 = __builtin_amdgcn_exp2f(m01[0] - M), f1 = __builtin_amdgcn_exp2f(m01[2] - M);
    const float f2 = __builtin_amdgcn_exp2f(m23[0] - M), f3 = __builtin_amdgcn_exp2f(m23[2] - M);
    const float Ls = fmaf(f0, m01[1], f1 * m01[3]) + fmaf(f2, m23[1], f3 * m23[3]);
    const LAS float* mg = (const LAS float*)(L + L_MRG) + head * 128 + 2 * lane;
    const f32x2 o0 = *(const LAS f32x2*)(mg), o1 = *(const LAS f32x2*)(mg + 512);
    const f32x2 o2 = *(const LAS f32x2*)(mg + 1024), o3 = *(const LAS f32x2*)(mg + 1536);
    const float rl = __builtin_amdgcn_rcpf(Ls);
    const f32x2 o = (f0 * o0 + f1 * o1 + (f2 * o2 + f3 * o3)) * rl;
    const unsigned lam = __float_as_uint(M + __builtin_amdgcn_logf(Ls));
    st_gran(ws, W_O + (unsigned)((((g * 4 + head) * 8 + (lane >> 3)) * 32 + s) * 8 + (lane & 7)) * 16,
            __float_as_uint(o[0]), tlo, __float_as_uint(o[1]), tb | ((lane & 1) ? (lam & 0xffffu) : (lam >> 16)));
  }
  STAMP(6);

  {
    u32x4 og;
    const unsigned off = W_O + (unsigned)(((g * 4 + (s >> 3)) * 8 + (s & 7)) * 256 + t) * 16;
    bool ok = false;
    for (unsigned spins = 0; spins < SPIN_CAP; ++spins) {
      asm volatile("" ::: "memory");
      if (!ok) og = ld_gran(ws, off);
      ok = og[1] == tlo && (og[3] & 0xffff0000u) == tb;
      if (__all(ok)) break;
      __builtin_amdgcn_s_sleep(POLL_SLEEP);
    }
    STAMP(7);
    const unsigned mine = og[3] & 0xffffu, other = dppu<XOR1>(mine);
    const float lam = __uint_as_float((lane & 1) ? ((other << 16) | mine) : ((mine << 16) | other));
    const float Mw = max_mod8(lam);
    const float wt = __builtin_amdgcn_exp2f(lam - Mw);
    const float a0 = sum_mod8(wt * __uint_as_float(og[0])), a1 = sum_mod8(wt * __uint_as_float(og[2])), Ww = sum_mod8(wt);
    LAS float* r2 = (LAS float*)(L + L_RED);
    if (lane < 8) { r2[(2 * lane) * 4 + cw] = a0; r2[(2 * lane + 1) * 4 + cw] = a1; }
    if (lane == 8) { r2[64 + cw] = Mw; r2[68 + cw] = Ww; }
    asm volatile("s_waitcnt lgkmcnt(0)" ::: "memory");
    __builtin_amdgcn_s_barrier();
    const f32x4 ov = *(const LAS f32x4*)(r2 + 4 * (lane & 15)), mv = *(const LAS f32x4*)(r2 + 64), wv = *(const LAS f32x4*)(r2 + 68);
    const float M = fmaxf(fmaxf(mv[0], mv[1]), fmaxf(mv[2], mv[3]));
    const float f0 = __builtin_amdgcn_exp2f(mv[0] - M), f1 = __builtin_amdgcn_exp2f(mv[1] - M);
    const float f2 = __builtin_amdgcn_exp2f(mv[2] - M), f3 = __builtin_amdgcn_exp2f(mv[3] - M);
    const float num = fmaf(f0, ov[0], f1 * ov[1]) + fmaf(f2, ov[2], f3 * ov[3]);
    const float den = fmaf(f0, wv[0], f1 * wv[1]) + fmaf(f2, wv[2], f3 * wv[3]);
    const float od = rbf(num * __builtin_amdgcn_rcpf(den));
    float s0 = 0.f, s1 = 0.f, s2 = 0.f, s3 = 0.f;
#pragma unroll
    for (int r = 0; r < 16; r += 2) {
      const float oa = __builtin_bit_cast(float, __builtin_amdgcn_readlane(__builtin_bit_cast(int, od), r));
      const float ob = __builtin_bit_cast(float, __builtin_amdgcn_readlane(__builtin_bit_cast(int, od), r + 1));
      s0 = fmaf(oa, wof[2 * r], s0); s1 = fmaf(oa, wof[2 * r + 1], s1);
      s2 = fmaf(ob, wof[2 * r + 2], s2); s3 = fmaf(ob, wof[2 * r + 3], s3);
    }
    st_gran(ws, W_S + (unsigned)(((t >> 2) * 256 + c) * 4 + (t & 3)) * 16, __float_as_uint(s0 + s2), tlo, __float_as_uint(s1 + s3), thi);
  }
  STAMP(8);

  if (s < 8) {
    u32x4 sg[4];
    const unsigned off = W_S + (unsigned)((fin * 256 + (t >> 2)) * 4 + (t & 3)) * 16;
    bool ok[4] = {false, false, false, false};
    for (unsigned spins = 0; spins < SPIN_CAP; ++spins) {
      asm volatile("" ::: "memory");
#pragma unroll
      for (int i = 0; i < 4; ++i) if (!ok[i]) sg[i] = ld_gran(ws, off + i * 64 * 64);
#pragma unroll
      for (int i = 0; i < 4; ++i) ok[i] = sg[i][1] == tlo && sg[i][3] == thi;
      if (__all(ok[0] && ok[1] && ok[2] && ok[3])) break;
      __builtin_amdgcn_s_sleep(POLL_SLEEP);
    }
    STAMP(9);
    const float a0 = sum_mod4((__uint_as_float(sg[0][0]) + __uint_as_float(sg[1][0])) + (__uint_as_float(sg[2][0]) + __uint_as_float(sg[3][0])));
    const float a1 = sum_mod4((__uint_as_float(sg[0][2]) + __uint_as_float(sg[1][2])) + (__uint_as_float(sg[2][2]) + __uint_as_float(sg[3][2])));
    LAS float* r3 = (LAS float*)(L + L_RED) + 128;
    if (lane < 4) { r3[(2 * lane) * 4 + cw] = a0; r3[(2 * lane + 1) * 4 + cw] = a1; }
    asm volatile("s_waitcnt lgkmcnt(0)" ::: "memory");
    __builtin_amdgcn_s_barrier();
    if (t < 4) {
      const f32x4 u0 = *(const LAS f32x4*)(r3 + 8 * t), u1 = *(const LAS f32x4*)(r3 + 8 * t + 4);
      const f32x2 gv = *(const LAS f32x2*)(L + L_RED + 768 + 8 * t);
      const float s0 = rbf((u0[0] + u0[1]) + (u0[2] + u0[3])), s1 = rbf((u1[0] + u1[1]) + (u1[2] + u1[3]));
      *(GAS unsigned*)(out + 8 * fin + 2 * t) = pack2(bflo(xo) + rbf(s0 * gv[0]), bfhi(xo) + rbf(s1 * gv[1]));
      if (w == 0 && t == 0) __builtin_amdgcn_raw_buffer_store_b64(u32x2{tlo, thi}, ws, W_EPOCH, 0, 16);
    }
    STAMP(10);
  }
  STAMP_FLUSH();
}

}

extern "C" void kernel_launch(void* const* d_in, const int* in_sizes, int n_in, void* d_out, int out_size, void* d_ws, size_t ws_size,
                              hipStream_t stream) {
  (void)in_sizes; (void)n_in; (void)out_size; (void)ws_size;
  (void)hipFuncSetAttribute((const void*)fused_decode_kernel, hipFuncAttributeMaxDynamicSharedMemorySize, L_END);
  hipLaunchKernelGGL(fused_decode_kernel, dim3(NWG), dim3(NTHR), L_END, stream,
                     (const unsigned short*)d_in[0], (const unsigned short*)d_in[1], (const unsigned short*)d_in[2],
                     (const unsigned short*)d_in[3], (const unsigned short*)d_in[4], (const unsigned short*)d_in[6],
                     (const unsigned short*)d_in[7], (unsigned short*)d_out, (unsigned char*)d_ws);
}
```

```cpp
#include <hip/hip_runtime.h>
#include <hip/hip_bf16.h>
#include <stdint.h>

#define LAS __attribute__((address_space(3)))
#define GAS __attribute__((address_space(1)))
typedef unsigned u32x4 __attribute__((ext_vector_type(4)));
typedef unsigned u32x2 __attribute__((ext_vector_type(2)));
typedef int i32x4 __attribute__((ext_vector_type(4)));
typedef float f32x4 __attribute__((ext_vector_type(4)));
typedef float f32x2 __attribute__((ext_vector_type(2)));
typedef __bf16 bf16x8 __attribute__((ext_vector_type(8)));
typedef __bf16 bf16x4 __attribute__((ext_vector_type(4)));
typedef __bf16 bf16x2 __attribute__((ext_vector_type(2)));
typedef short s16x4 __attribute__((ext_vector_type(4)));

namespace {

constexpr int DM = 512, HKV = 8, SK = 65536, DH = 128, DFF = 1792;
constexpr int NWG = 256, NTHR = 384;
#define MAP_SPREAD 1
#define POOLPER 14
#define XBACK 26
#define XPOLL 10
#define SIDE_TAU 32
#define TAU 28
constexpr int POOL = 32 * POOLPER;
#define FFN_SHED 14
#define RING_ 16
constexpr int RING = RING_, SLOT = 8192;
#define INFL_ 3
#define KPOL "nt"
#define VPOL "nt"
#define WPOL "nt"
#define BACK_HI 32
#define BACK_LO 12
#define BACK_SLEEP_HI 32
#define BACK_SLEEP_LO 10
#define AUX_PRIO 3
#define CSPIN_SLEEP 0
#define AUX_SLEEP 8
#define AUX_LEAD 0
#define NBURST 8
#define SIDE2_FIRST 24
#define AUX_FIRST 12
#define AUX_STEP 6
#define WAITN_ (8 * (INFL_ - 1))
constexpr int WAITN = WAITN_;
constexpr int INFL = (WAITN + 7) / 8 + 1;
constexpr int NJOB = DFF / 64;
#define GPF_ 4
constexpr int GPF = GPF_;
constexpr int NFIN = 256 / GPF;
constexpr int FPG = NFIN / 8;
constexpr unsigned KV_BYTES = (unsigned)HKV * SK * DH * 2;

constexpr int L_RING = 0;
constexpr int L_WO = RING * SLOT;
constexpr int L_CTRL = L_WO + 16384;
constexpr int L_XN = L_CTRL + 256;
constexpr int L_Q = L_XN + 2048;
constexpr int L_RED = L_Q + 1024;
constexpr int L_FT = L_RED + 1024;
constexpr int L_MRG = L_FT + 2048;
constexpr int L_ML = L_MRG + 8192;
constexpr int L_END = L_ML + 256;
static_assert(L_END <= 160 * 1024, "LDS");

constexpr unsigned W_EPOCH = 0;
constexpr unsigned W_Q = 4096;
constexpr unsigned W_O = W_Q + 8 * 8 * 4 * 32 * 16;
constexpr unsigned W_S = W_O + 8 * 4 * 8 * 32 * 8 * 16;
constexpr unsigned W_F = W_S + 256 * 256 * 16;
constexpr unsigned W_T = W_F + NJOB * 256 * 16;
constexpr unsigned W_END = W_T + 8 * 32 * 16;

constexpr unsigned SPIN_CAP = 1u << 20;
#define LDSU(x) ((unsigned)__builtin_amdgcn_readfirstlane(x))
#define NPRE_ 3
constexpr int NPRE = NPRE_;
#define LDELAY 0
#define POLL_SLEEP 2

#define STAMP(i) do {} while (0)
#define STAMP_FLUSH() do {} while (0)

__device__ __forceinline__ float bf2f(unsigned short b) { return __builtin_bit_cast(float, (unsigned)b << 16); }
__device__ __forceinline__ float bflo(unsigned u) { return __builtin_bit_cast(float, u << 16); }
__device__ __forceinline__ float bfhi(unsigned u) { return __builtin_bit_cast(float, u & 0xffff0000u); }
__device__ __forceinline__ unsigned short f2bf(float f) { return __builtin_bit_cast(unsigned short, (__bf16)f); }
__device__ __forceinline__ float rbf(float f) { return bf2f(f2bf(f)); }
__device__ __forceinline__ unsigned pack2(float lo, float hi) {
  const f32x2 v = {lo, hi};
  return __builtin_bit_cast(unsigned, __builtin_convertvector(v, bf16x2));
}

template <int CTRL> __device__ __forceinline__ float dpp(float x) {
  return __builtin_bit_cast(float, __builtin_amdgcn_mov_dpp(__builtin_bit_cast(int, x), CTRL, 0xf, 0xf, true));
}
template <int CTRL> __device__ __forceinline__ unsigned dppu(unsigned x) {
  return (unsigned)__builtin_amdgcn_mov_dpp((int)x, CTRL, 0xf, 0xf, true);
}
constexpr int XOR1 = 0xB1, XOR2 = 0x4E, ROR4 = 0x124, ROR8 = 0x128;
__device__ __forceinline__ float xrow_sum(float x) {
  auto s = __builtin_amdgcn_permlane16_swap(__float_as_uint(x), __float_as_uint(x), false, false);
  x = __uint_as_float(s[0]) + __uint_as_float(s[1]);
  auto t = __builtin_amdgcn_permlane32_swap(__float_as_uint(x), __float_as_uint(x), false, false);
  return __uint_as_float(t[0]) + __uint_as_float(t[1]);
}
__device__ __forceinline__ float xrow_max(float x) {
  auto s = __builtin_amdgcn_permlane16_swap(__float_as_uint(x), __float_as_uint(x), false, false);
  x = fmaxf(__uint_as_float(s[0]), __uint_as_float(s[1]));
  auto t = __builtin_amdgcn_permlane32_swap(__float_as_uint(x), __float_as_uint(x), false, false);
  return fmaxf(__uint_as_float(t[0]), __uint_as_float(t[1]));
}
__device__ __forceinline__ float wave_sum(float x) {
  x += dpp<XOR1>(x); x += dpp<XOR2>(x); x += dpp<ROR4>(x); x += dpp<ROR8>(x);
  return xrow_sum(x);
}
__device__ __forceinline__ float sum_mod2(float x) {
  x += dpp<XOR2>(x); x += dpp<ROR4>(x); x += dpp<ROR8>(x);
  return xrow_sum(x);
}
__device__ __forceinline__ float sum_mod4(float x) {
  x += dpp<ROR4>(x); x += dpp<ROR8>(x);
  return xrow_sum(x);
}
__device__ __forceinline__ float sum_mod8(float x) { x += dpp<ROR8>(x); return xrow_sum(x); }
__device__ __forceinline__ float max_mod8(float x) { x = fmaxf(x, dpp<ROR8>(x)); return xrow_max(x); }

__device__ __forceinline__ u32x4 ld_nt16(const void* p) { return __builtin_nontemporal_load((const GAS u32x4*)p); }

__device__ __forceinline__ void poll_pause(bool missing) {
  const int n = __builtin_popcountll(__ballot(missing));
  if (n > BACK_HI) __builtin_amdgcn_s_sleep(BACK_SLEEP_HI);
  else if (n > BACK_LO) __builtin_amdgcn_s_sleep(BACK_SLEEP_LO);
  else __builtin_amdgcn_s_sleep(POLL_SLEEP);
}

__device__ __forceinline__ void st_gran(__amdgpu_buffer_rsrc_t ws, unsigned off, unsigned a, unsigned b, unsigned c, unsigned d) {
  const u32x4 g = {a, b, c, d};
  __builtin_amdgcn_raw_buffer_store_b128(g, ws, off, 0, 16);
}
__device__ __forceinline__ u32x4 ld_gran(__amdgpu_buffer_rsrc_t ws, unsigned off) {
  return __builtin_amdgcn_raw_buffer_load_b128(ws, off, 0, 16);
}
__device__ __forceinline__ void poll_gran(__amdgpu_buffer_rsrc_t ws, unsigned off, bool have, u32x4& g) {
  const u32x4 v = __builtin_amdgcn_raw_buffer_load_b128(ws, have ? 0x7ffffff0u : off, 0, 16);
#pragma unroll
  for (int k = 0; k < 4; ++k) g[k] = have ? g[k] : v[k];
}

__device__ __forceinline__ int job_of(int g, int s) { return g * 4 + (s >> 3); }

__global__ void __launch_bounds__(NTHR, 2)
fused_decode_kernel(const unsigned short* __restrict__ x, const unsigned short* __restrict__ kc, const unsigned short* __restrict__ vc,
                    const unsigned short* __restrict__ wq, const unsigned short* __restrict__ wo, const unsigned short* __restrict__ w1,
                    const unsigned short* __restrict__ w2, unsigned short* __restrict__ out, unsigned char* __restrict__ wsp) {
  extern __shared__ __attribute__((aligned(16))) unsigned char lds[];
  LAS unsigned char* L = (LAS unsigned char*)lds;
  volatile LAS unsigned* ctrl = (volatile LAS unsigned*)(L + L_CTRL);
  const int w = blockIdx.x;
  const int g = w >> 5, s = 4 * (w & 7) + ((w >> 3) & 3);
  const int wave = __builtin_amdgcn_readfirstlane(threadIdx.x >> 6), lane = threadIdx.x & 63;
  constexpr int NF_J = 128 - FFN_SHED, NF_O = 128 + FFN_SHED / 7;
  const bool has_job = g < 7 && (s & 7) == 2;
  auto first_fill = [&](int i) -> int { return g == 7 ? 128 * i : NF_O * i - (NF_O - NF_J) * ((i + 5) >> 3); };
  const int fill0 = first_fill(s);
  const int nstat = (g == 7 ? 128 : has_job ? NF_J : NF_O) - POOLPER;
  STAMP(0);

  if (wave == 0) {
    const unsigned long long kb = (unsigned long long)kc, vb = (unsigned long long)vc;
    const i32x4 sk = {(int)kb, (int)(kb >> 32), (int)KV_BYTES, 0x00020000};
    const i32x4 sv = {(int)vb, (int)(vb >> 32), (int)KV_BYTES, 0x00020000};
    const unsigned base = (unsigned)g * (SK * DH * 2) + (unsigned)fill0 * 4096;
    const unsigned r4 = lane >> 4, p = lane & 15;
    const unsigned k0 = r4 * 256 + 16 * (p ^ (0 + r4)), k1 = r4 * 256 + 16 * (p ^ (4 + r4));
    const unsigned k2 = r4 * 256 + 16 * (p ^ (8 + r4)), k3 = r4 * 256 + 16 * (p ^ (12 + r4));
    const unsigned v0 = r4 * 256 + 16 * (p ^ (r4 << 1)), v1 = r4 * 256 + 16 * (p ^ ((4 + r4) << 1));
    const unsigned ring0 = (unsigned)(size_t)(L + L_RING);
    __builtin_amdgcn_s_setprio(3);
    const int naux = has_job ? 20 : 4, job = job_of(g, s);
    const unsigned vq = (lane >> 3) * 8192 + (lane & 7) * 16, v1w = (lane >> 3) * (DFF * 2) + (lane & 7) * 16, vc16 = lane * 16;
    const unsigned aux0 = (unsigned)(size_t)(L + L_WO);
    const unsigned long long qb = (unsigned long long)wq, ob = (unsigned long long)wo, b1 = (unsigned long long)w1, b2 = (unsigned long long)w2;
    const i32x4 sq = {(int)qb, (int)(qb >> 32), DM * 4096 * 2, 0x00020000}, so = {(int)ob, (int)(ob >> 32), 4096 * DM * 2, 0x00020000};
    const i32x4 s1 = {(int)b1, (int)(b1 >> 32), DM * DFF * 2, 0x00020000}, s2 = {(int)b2, (int)(b2 >> 32), DM * DFF * 2, 0x00020000};
    const __amdgpu_buffer_rsrc_t wsr = __builtin_amdgcn_make_buffer_rsrc((void*)wsp, 0, (int)W_END, 0x00020000);
    unsigned long long epl;
    asm volatile("global_load_dwordx2 %0, %1, %2 sc1" : "=v"(epl) : "v"(0u), "s"(wsp) : "memory");
    if (lane < 16) ctrl[lane] = 0u;
    asm volatile("s_waitcnt lgkmcnt(0)" ::: "memory");
    __builtin_amdgcn_s_barrier();
    if (LDELAY) __builtin_amdgcn_s_sleep(LDELAY);
#define PARK_BEGIN
#define PARK_END
#define PROGRESS
#define KV_ISSUE(WAIT) asm volatile( \
            WAIT "s_mov_b32 m0, %0\n\ts_nop 0\n\t" \
            "buffer_load_dwordx4 %2, %8, %10 offen " KPOL " lds\n\t" \
            "buffer_load_dwordx4 %3, %8, %10 offen offset:1024 " KPOL " lds\n\t" \
            "buffer_load_dwordx4 %4, %8, %10 offen offset:2048 " KPOL " lds\n\t" \
            "buffer_load_dwordx4 %5, %8, %10 offen offset:3072 " KPOL " lds\n\t" \
            "s_mov_b32 m0, %1\n\ts_nop 0\n\t" \
            "buffer_load_dwordx4 %6, %9, %10 offen " VPOL " lds\n\t" \
            "buffer_load_dwordx4 %7, %9, %10 offen offset:1024 " VPOL " lds\n\t" \
            "buffer_load_dwordx4 %6, %9, %10 offen offset:2048 " VPOL " lds\n\t" \
            "buffer_load_dwordx4 %7, %9, %10 offen offset:3072 " VPOL " lds" \
            :: "s"(m0k), "s"(m0k + 4096), "v"(k0), "v"(k1), "v"(k2), "v"(k3), "v"(v0), "v"(v1), "s"(sk), "s"(sv), "s"(sko), \
               "i"(WAITN) : "memory")
#define SIDE_ISSUE(SRD, VO, SO0, RSTEP) do { \
          const unsigned so0_ = (SO0), st_ = (RSTEP) - 1024, so4_ = so0_ + 4 * (RSTEP); \
          asm volatile( \
            "s_waitcnt vmcnt(%12)\n\t" \
            "s_mov_b32 m0, %0\n\ts_nop 0\n\t" \
            "buffer_load_dwordx4 %2, %3, %4 offen " WPOL " lds\n\t" \
            "buffer_load_dwordx4 %2, %3, %5 offen offset:1024 " WPOL " lds\n\t" \
            "buffer_load_dwordx4 %2, %3, %6 offen offset:2048 " WPOL " lds\n\t" \
            "buffer_load_dwordx4 %2, %3, %7 offen offset:3072 " WPOL " lds\n\t" \
            "s_mov_b32 m0, %1\n\ts_nop 0\n\t" \
            "buffer_load_dwordx4 %2, %3, %8 offen " WPOL " lds\n\t" \
            "buffer_load_dwordx4 %2, %3, %9 offen offset:1024 " WPOL " lds\n\t" \
            "buffer_load_dwordx4 %2, %3, %10 offen offset:2048 " WPOL " lds\n\t" \
            "buffer_load_dwordx4 %2, %3, %11 offen offset:3072 " WPOL " lds" \
            :: "s"(dst), "s"(dst + 4096), "v"(VO), "s"(SRD), "s"(so0_), "s"(so0_ + st_), "s"(so0_ + 2 * st_), "s"(so0_ + 3 * st_), \
               "s"(so4_), "s"(so4_ + st_), "s"(so4_ + 2 * st_), "s"(so4_ + 3 * st_), "i"(WAITN) : "memory"); } while (0)
    auto side_pos = [&](int a) -> int { return a >= naux ? 0x7fffffff : a < 2 ? a : has_job ? AUX_FIRST + AUX_STEP * (a - 2) : SIDE2_FIRST + 6 * (a - 2); };
    unsigned m0k = ring0, sko = base;
#pragma unroll
    for (int a = 0; a < 2; ++a) {
      const unsigned dst = aux0 + (unsigned)a * 8192;
      SIDE_ISSUE(sq, vq, (unsigned)(128 * (s >> 3) + 64 * a) * 8192 + g * 1024 + (s & 7) * 128, 8 * 8192u);
    }
#pragma unroll
    for (int j = 0; j < NBURST - 2; ++j) { KV_ISSUE(""); m0k += SLOT; sko += 4096; }
    asm volatile("s_waitcnt vmcnt(%0)" :: "i"(8 * (NBURST - 2)) : "memory");
    *(volatile LAS u32x2*)(L + L_CTRL) = u32x2{0u, 2u};
    asm volatile("" : "+v"(epl));
    const unsigned long long tagl = epl + 1ull;
    const unsigned tlo = __builtin_amdgcn_readfirstlane((unsigned)tagl), thi = __builtin_amdgcn_readfirstlane((unsigned)(tagl >> 32));
    int kv = NBURST - 2, ax = 2, e = NBURST, le = 2, axl = 2, lim = RING, alim = 2;
    int nextpos = side_pos(2), nextland = nextpos;
    const int nent = nstat + naux;
    const unsigned ring_end = ring0 + RING * SLOT;
    u32x4 dn = {0u, 0u, 0u, 0u};
    unsigned da = 0u;
#define ACCOUNT() do { \
        le = max(le, e + 1 - INFL); \
        if (le > nextland) { ++axl; nextland = side_pos(axl); } \
        *(volatile LAS u32x2*)(L + L_CTRL) = u32x2{(unsigned)(le - axl), (unsigned)axl}; \
        lim = max(lim, RING + 4 * (int)LDSU(min(min(dn[0], dn[1]), min(dn[2], dn[3])))); \
        alim = max(alim, 2 + (int)LDSU(da)); \
        dn = *(volatile LAS u32x4*)(L + L_CTRL + 16); \
        da = ctrl[3]; \
      } while (0)
#define PARK(COND) do { \
        asm volatile("s_waitcnt vmcnt(0)" ::: "memory"); \
        le = e; while (le > nextland) { ++axl; nextland = side_pos(axl); } \
        *(volatile LAS u32x2*)(L + L_CTRL) = u32x2{(unsigned)(le - axl), (unsigned)axl}; \
        PARK_BEGIN \
        for (unsigned spins = 0; spins < SPIN_CAP; ++spins) { if (COND) break; __builtin_amdgcn_s_sleep(1); } \
        PARK_END \
        asm volatile("" ::: "memory"); \
      } while (0)
#define KV_STEP() do { \
        if (kv >= lim) { PARK(LDSU(ctrl[4 + ((kv - RING) & 3)]) > (unsigned)((kv - RING) >> 2)); lim = kv + 1; } \
        KV_ISSUE("s_waitcnt vmcnt(%11)\n\t"); \
        m0k += SLOT; if (m0k == ring_end) m0k = ring0; \
        sko += 4096; \
        ++kv; \
        ACCOUNT(); \
        PROGRESS \
      } while (0)
    bool stamped = false;
    while (e < nent) {
      const int stop = min(nextpos, nent);
      for (; e < stop; ++e) {
        KV_STEP();
        if (kv == nstat - XBACK) {
          const unsigned now = (unsigned)__builtin_amdgcn_s_memrealtime() + (unsigned)((naux - ax) * SIDE_TAU);
          if (lane == 0) st_gran(wsr, W_T + (unsigned)(g * 32 + s) * 16, now, tlo, 0u, thi);
          stamped = true;
        }
      }
      if (e < nent) {
        if (ax >= alim) { PARK(LDSU(ctrl[3]) + 2u > (unsigned)ax); alim = ax + 1; }
        const unsigned dst = aux0 + (unsigned)(ax & 1) * 8192;
        if (ax >= naux - 2) SIDE_ISSUE(so, vc16, (unsigned)(16 * (g * 32 + s) + 8 * (ax - (naux - 2))) * 1024, 1024u);
        else if (ax < 10) SIDE_ISSUE(s1, v1w, (unsigned)(64 * (ax - 2)) * (DFF * 2) + job * 128, 8u * DFF * 2);
        else SIDE_ISSUE(s2, vc16, (unsigned)(64 * job + 8 * (ax - 10)) * 1024, 1024u);
        ++ax;
        nextpos = side_pos(ax);
        ACCOUNT();
        ++e;
      }
    }
    (void)stamped;
    if (LDSU(ctrl[13]) == 0u) PARK(LDSU(ctrl[13]) != 0u);
    const int share = (int)LDSU(ctrl[14]);
    ctrl[2] = (unsigned)(kv + share);
    for (int tix = (int)LDSU(ctrl[13]) - 1, stop = tix + share; tix < stop; ++tix, ++e) {
      const int gap = tix / POOLPER;
      sko = (unsigned)g * (SK * DH * 2) + (unsigned)(first_fill(gap + 1) - POOLPER + (tix - gap * POOLPER)) * 4096;
      KV_STEP();
    }
#pragma unroll
    for (int r = INFL - 1; r >= 0; --r) {
      asm volatile("s_waitcnt vmcnt(%0)" :: "i"(8 * r) : "memory");
      le = max(le, e - r);
      while (le > nextland) { ++axl; nextland = side_pos(axl); }
      *(volatile LAS u32x2*)(L + L_CTRL) = u32x2{(unsigned)(le - axl), (unsigned)axl};
    }
#undef KV_STEP
#undef KV_ISSUE
#undef SIDE_ISSUE
#undef ACCOUNT
#undef PARK
    STAMP(2);
    STAMP_FLUSH();
    return;
  }

  const __amdgpu_buffer_rsrc_t ws = __builtin_amdgcn_make_buffer_rsrc((void*)wsp, 0, (int)W_END, 0x00020000);
  const int c = g * 32 + s;
  const bool is_fin = s % (32 / FPG) == 1;
  const int fin = g * FPG + s / (32 / FPG);

  if (wave == 5) {
    const u32x2 ep = __builtin_amdgcn_raw_buffer_load_b64(ws, W_EPOCH, 0, 16);
    unsigned touch;
    {
      const unsigned char* p = (const unsigned char*)x;
      if (lane == 1) p = (const unsigned char*)wo + (size_t)c * 16384;
      if (lane == 2) p = (const unsigned char*)w1 + (size_t)(has_job ? job_of(g, s) * 128 : 0);
      if (lane == 3) p = (const unsigned char*)w1 + (size_t)(DM - 1) * DFF * 2;
      if (lane == 4) p = (const unsigned char*)w2 + (size_t)(has_job ? job_of(g, s) * 65536 : 0);
      if (lane == 5) p = wsp + W_O + (size_t)g * 131072;
      if (lane == 6) p = wsp + W_S;
      if (lane == 7) p = wsp + W_END - 4;
      if (lane == 8) p = (const unsigned char*)out;
      touch = *(const GAS unsigned*)p;
    }
    __builtin_amdgcn_s_barrier();
    asm volatile("" :: "v"(touch));
    for (unsigned spins = 0; LDSU(ctrl[8]) < 4u && spins < SPIN_CAP; ++spins) __builtin_amdgcn_s_sleep(4);
    asm volatile("" ::: "memory");
    const unsigned long long tag = (((unsigned long long)ep[1] << 32) | ep[0]) + 1ull;
    const unsigned tlo = __builtin_amdgcn_readfirstlane((unsigned)tag), thi = __builtin_amdgcn_readfirstlane((unsigned)(tag >> 32));
    if (has_job) {
      const int job = job_of(g, s);
      const int r8 = lane >> 3, cq = lane & 7;
      float h[8];
#pragma unroll
      for (int k = 0; k < 8; ++k) h[k] = 0.f;
#pragma unroll 1
      for (int p = 0; p < 8; ++p) {
        for (unsigned spins = 0; LDSU(ctrl[1]) < (unsigned)(3 + p) && spins < SPIN_CAP; ++spins) __builtin_amdgcn_s_sleep(4);
        asm volatile("" ::: "memory");
        const LAS unsigned char* src = L + L_WO + (p & 1) * 8192 + r8 * 128 + cq * 16;
#pragma unroll
        for (int i = 0; i < 8; ++i) {
          const u32x4 wr = *(const LAS u32x4*)(src + i * 1024);
          const float xn = *(const LAS float*)(L + L_XN + 4 * (64 * p + 8 * i + r8));
#pragma unroll
          for (int k = 0; k < 4; ++k) { h[2 * k] = fmaf(xn, bflo(wr[k]), h[2 * k]); h[2 * k + 1] = fmaf(xn, bfhi(wr[k]), h[2 * k + 1]); }
        }
        asm volatile("s_waitcnt lgkmcnt(0)" ::: "memory");
        if (lane == 0) ctrl[3] = (unsigned)(3 + p);
      }
#pragma unroll
      for (int k = 0; k < 8; ++k) h[k] = fmaxf(rbf(sum_mod8(h[k])), 0.f);
      float bp[8];
#pragma unroll
      for (int k = 0; k < 8; ++k) bp[k] = 0.f;
#pragma unroll 1
      for (int p = 0; p < 8; ++p) {
        for (unsigned spins = 0; LDSU(ctrl[1]) < (unsigned)(11 + p) && spins < SPIN_CAP; ++spins) __builtin_amdgcn_s_sleep(4);
        asm volatile("" ::: "memory");
        const LAS unsigned char* src = L + L_WO + (p & 1) * 8192 + lane * 16;
#pragma unroll
        for (int i = 0; i < 8; ++i) {
          const u32x4 wr = *(const LAS u32x4*)(src + i * 1024);
          const float hv = __builtin_bit_cast(float, __builtin_amdgcn_readlane(__builtin_bit_cast(int, h[i]), p));
#pragma unroll
          for (int k = 0; k < 4; ++k) { bp[2 * k] = fmaf(hv, bflo(wr[k]), bp[2 * k]); bp[2 * k + 1] = fmaf(hv, bfhi(wr[k]), bp[2 * k + 1]); }
        }
        asm volatile("s_waitcnt lgkmcnt(0)" ::: "memory");
        if (lane == 0) ctrl[3] = (unsigned)(11 + p);
      }
      *(LAS f32x4*)(L + L_FT + 32 * lane) = f32x4{bp[0], bp[1], bp[2], bp[3]};
      *(LAS f32x4*)(L + L_FT + 32 * lane + 16) = f32x4{bp[4], bp[5], bp[6], bp[7]};
      asm volatile("s_waitcnt lgkmcnt(0)" ::: "memory");
#pragma unroll
      for (int e = 0; e < 4; ++e) {
        const f32x2 v = *(const LAS f32x2*)(L + L_FT + 8 * (64 * e + lane));
        st_gran(ws, W_F + (unsigned)(job * 256 + 64 * e + lane) * 16, __float_as_uint(v[0]), tlo, __float_as_uint(v[1]), thi);
      }
    }
    {
      for (unsigned spins = 0; (int)LDSU(ctrl[0]) < nstat - XPOLL && spins < SPIN_CAP; ++spins) __builtin_amdgcn_s_sleep(4);
      u32x4 tg = {};
      bool ok = false;
      for (unsigned spins = 0; spins < SPIN_CAP; ++spins) {
        asm volatile("" ::: "memory");
        poll_gran(ws, W_T + (unsigned)(g * 32 + (lane & 31)) * 16, ok, tg);
        ok = tg[1] == tlo && tg[3] == thi;
        if (__all(ok)) break;
        __builtin_amdgcn_s_sleep(24);
      }
      const int d = (int)(tg[0] - (unsigned)__builtin_amdgcn_readlane((int)tg[0], 0));
      const float mean = wave_sum((float)d) * (1.f / 64.f);
      const float a = floorf(fmaxf((float)(POOLPER * TAU) + (mean - (float)d), 0.f));
      float pre = lane < 32 ? a : 0.f;
      pre += __builtin_bit_cast(float, __builtin_amdgcn_update_dpp(0, __builtin_bit_cast(int, pre), 0x111, 0xf, 0xf, true));
      pre += __builtin_bit_cast(float, __builtin_amdgcn_update_dpp(0, __builtin_bit_cast(int, pre), 0x112, 0xf, 0xf, true));
      pre += __builtin_bit_cast(float, __builtin_amdgcn_update_dpp(0, __builtin_bit_cast(int, pre), 0x114, 0xf, 0xf, true));
      pre += __builtin_bit_cast(float, __builtin_amdgcn_update_dpp(0, __builtin_bit_cast(int, pre), 0x118, 0xf, 0xf, true));
      pre += __builtin_bit_cast(float, __builtin_amdgcn_update_dpp(0, __builtin_bit_cast(int, pre), 0x142, 0xa, 0xf, false));
      const float tot = __builtin_bit_cast(float, __builtin_amdgcn_readlane(__builtin_bit_cast(int, pre), 31));
      const float scale = tot > 0.f ? (float)POOL / tot : 0.f;
      const int bnd = lane == 31 ? POOL : min((int)(pre * scale), POOL);
      const int hi = __builtin_amdgcn_readlane(bnd, s), lo = s == 0 ? 0 : __builtin_amdgcn_readlane(bnd, s - 1);
      if (lane == 0) ctrl[14] = (unsigned)(hi - lo);
      asm volatile("s_waitcnt lgkmcnt(0)" ::: "memory");
      if (lane == 0) ctrl[13] = (unsigned)(lo + 1);
    }
    STAMP(1);
    if (is_fin) for (unsigned spins = 0; spins < SPIN_CAP; ++spins) { const u32x4 cv = *(volatile LAS u32x4*)(L + L_CTRL); if (LDSU(cv[2]) != 0u && LDSU(cv[0]) >= LDSU(cv[2])) break; __builtin_amdgcn_s_sleep(AUX_SLEEP); }
    if (is_fin) {
      constexpr int JPL = 64 / GPF, NL = (NJOB + JPL - 1) / JPL;
      u32x4 fg[NL];
      const unsigned oa = W_F + (unsigned)((lane / GPF) * 256 + fin * GPF + (lane % GPF)) * 16;
      for (unsigned spins = 0; spins < SPIN_CAP; ++spins) {
        asm volatile("" ::: "memory");
        bool ok = true;
#pragma unroll
        for (int i = 0; i < NL; ++i) fg[i] = ld_gran(ws, lane / GPF + JPL * i < NJOB ? oa + i * JPL * 256 * 16 : oa);
#pragma unroll
        for (int i = 0; i < NL; ++i) ok = ok && fg[i][1] == tlo && fg[i][3] == thi;
        if (__all(ok)) break;
        __builtin_amdgcn_s_sleep(8);
      }
      float b0 = 0.f, b1 = 0.f;
#pragma unroll
      for (int i = 0; i < NL; ++i) if (lane / GPF + JPL * i < NJOB) { b0 += __uint_as_float(fg[i][0]); b1 += __uint_as_float(fg[i][2]); }
      b0 = rbf(GPF == 4 ? sum_mod4(b0) : sum_mod8(b0)); b1 = rbf(GPF == 4 ? sum_mod4(b1) : sum_mod8(b1));
      const float g0 = rbf(b0 / (1.f + __expf(-b0))), g1 = rbf(b1 / (1.f + __expf(-b1)));
      if (lane < GPF) *(LAS f32x2*)(L + L_RED + 768 + 8 * lane) = f32x2{g0, g1};
      asm volatile("s_waitcnt lgkmcnt(0)" ::: "memory");
    }
    STAMP_FLUSH();
    return;
  }

  const int cw = wave - 1, t = threadIdx.x - 64;

  const u32x2 ep = __builtin_amdgcn_raw_buffer_load_b64(ws, W_EPOCH, 0, 16);
  const u32x4 xr = *(const GAS u32x4*)(x + 8 * lane);
  unsigned xo = *(const GAS unsigned*)(x + 2 * GPF * fin + 2 * (lane % GPF));
  const int trow = 32 * cw + (lane >> 3);
  __builtin_amdgcn_s_barrier();

  const unsigned long long tag = (((unsigned long long)ep[1] << 32) | ep[0]) + 1ull;
  const unsigned tlo = __builtin_amdgcn_readfirstlane((unsigned)tag), thi = __builtin_amdgcn_readfirstlane((unsigned)(tag >> 32));

  {
    float xf[8];
#pragma unroll
    for (int k = 0; k < 4; ++k) { xf[2 * k] = bflo(xr[k]); xf[2 * k + 1] = bfhi(xr[k]); }
    float sum = ((xf[0] + xf[1]) + (xf[2] + xf[3])) + ((xf[4] + xf[5]) + (xf[6] + xf[7]));
    const float mean = wave_sum(sum) * (1.f / DM);
    float sq = 0.f;
#pragma unroll
    for (int k = 0; k < 8; ++k) { const float d = xf[k] - mean; sq = fmaf(d, d, sq); }
    const float var = wave_sum(sq) * (1.f / DM);
    const float mb = rbf(mean), sd = rbf(__builtin_amdgcn_sqrtf(rbf(rbf(var) + 1e-5f)));
    const float rs = __builtin_amdgcn_rcpf(sd);
    f32x4 a, b;
#pragma unroll
    for (int k = 0; k < 4; ++k) { a[k] = rbf(rbf(xf[k] - mb) * rs); b[k] = rbf(rbf(xf[4 + k] - mb) * rs); }
    *(LAS f32x4*)(L + L_XN + 32 * lane) = a;
    *(LAS f32x4*)(L + L_XN + 32 * lane + 16) = b;
  }
  asm volatile("s_waitcnt lgkmcnt(0)" : "+v"(xo) :: "memory");
  float xq[4];
#pragma unroll
  for (int i = 0; i < 4; ++i) xq[i] = *(const LAS float*)(L + L_XN + 4 * (128 * (s >> 3) + trow + 8 * i));
  STAMP(1);
  for (unsigned spins = 0; LDSU(ctrl[1]) < 2u && spins < SPIN_CAP; ++spins) {}
  asm volatile("" ::: "memory");
  u32x4 wqr[4];
#pragma unroll
  for (int i = 0; i < 4; ++i) wqr[i] = *(const LAS u32x4*)(L + L_WO + (trow + 8 * i) * 128 + (lane & 7) * 16);

  LAS float* red = (LAS float*)(L + L_RED);
  {
    float acc[8];
#pragma unroll
    for (int k = 0; k < 8; ++k) acc[k] = 0.f;
#pragma unroll
    for (int i = 0; i < 4; ++i)
#pragma unroll
      for (int k = 0; k < 4; ++k) {
        acc[2 * k] = fmaf(xq[i], bflo(wqr[i][k]), acc[2 * k]);
        acc[2 * k + 1] = fmaf(xq[i], bfhi(wqr[i][k]), acc[2 * k + 1]);
      }
#pragma unroll
    for (int k = 0; k < 8; ++k) acc[k] = sum_mod8(acc[k]);
    if (lane < 8) {
      *(LAS f32x4*)(red + cw * 64 + 8 * lane) = f32x4{acc[0], acc[1], acc[2], acc[3]};
      *(LAS f32x4*)(red + cw * 64 + 8 * lane + 4) = f32x4{acc[4], acc[5], acc[6], acc[7]};
    }
  }
  asm volatile("s_waitcnt lgkmcnt(0)" ::: "memory");
  if (lane == 0) __hip_atomic_fetch_add((LAS unsigned*)(ctrl + 8), 1u, __ATOMIC_RELAXED, __HIP_MEMORY_SCOPE_WORKGROUP);
  if (cw == 0) {
    for (unsigned spins = 0; LDSU(ctrl[8]) < 4u && spins < SPIN_CAP; ++spins) {}
    asm volatile("" ::: "memory");
    if (lane == 0) ctrl[3] = 2u;
    if (lane < 32) {
      const f32x2 q2 = (*(const LAS f32x2*)(red + 2 * lane) + *(const LAS f32x2*)(red + 64 + 2 * lane)) +
                       (*(const LAS f32x2*)(red + 128 + 2 * lane) + *(const LAS f32x2*)(red + 192 + 2 * lane));
      st_gran(ws, W_Q + (unsigned)(((g * 8 + (s & 7)) * 4 + (s >> 3)) * 32 + lane) * 16, __float_as_uint(q2[0]), tlo, __float_as_uint(q2[1]), thi);
    }
  }
  STAMP(2);
  unsigned ka[4], va[8];
  {
    const unsigned rho = lane & 15, gg = lane >> 4;
    const unsigned ring0 = (unsigned)(size_t)(L + L_RING);
#pragma unroll
    for (int m = 0; m < 4; ++m) ka[m] = ring0 + rho * 256 + 16 * ((4 * m + gg) ^ rho);
    const unsigned q = (lane & 15) >> 2, p = lane & 3, row = 4 * gg + q;
#pragma unroll
    for (int b = 0; b < 8; ++b) va[b] = ring0 + 4096 + row * 256 + 8 * p + 32 * (b ^ (row & 7));
  }
  int slot = cw;
  auto await = [&](int k) -> bool {
    const unsigned j = 4 * k + cw;
    for (unsigned spins = 0; spins < SPIN_CAP; ++spins) {
      const u32x4 cv = *(volatile LAS u32x4*)(L + L_CTRL);
      if (LDSU(cv[0]) > j) return true;
      const unsigned tot = LDSU(cv[2]);
      if (tot != 0u && j >= tot) return false;
      __builtin_amdgcn_s_sleep(1);
    }
    return false;
  };
  auto fetch = [&](bf16x8 (&kf)[4], bf16x4 (&vf)[8]) {
    asm volatile("" ::: "memory");
    const unsigned sb = (unsigned)slot * SLOT;
    slot += 4; if (slot >= RING) slot -= RING;
#pragma unroll
    for (int m = 0; m < 4; ++m) kf[m] = *(const LAS bf16x8*)(size_t)(ka[m] + sb);
#pragma unroll
    for (int b = 0; b < 8; ++b) vf[b] = __builtin_amdgcn_ds_read_tr16_b64_v4bf16((LAS bf16x4*)(size_t)(va[b] + sb));
  };
  auto release = [&](int k) {
    asm volatile("s_waitcnt lgkmcnt(0)" ::: "memory");
    if (lane == 0) ctrl[4 + cw] = (unsigned)k + 1u;
  };
  bf16x8 kp[NPRE][4];
  bf16x4 vp[NPRE][8];
#pragma unroll
  for (int k = 0; k < NPRE; ++k) { (void)await(k); fetch(kp[k], vp[k]); release(k); }
#pragma unroll
  for (int k = 0; k < NPRE; ++k) {
#pragma unroll
    for (int m = 0; m < 4; ++m) asm volatile("" : "+v"(kp[k][m]));
#pragma unroll
    for (int b = 0; b < 8; ++b) asm volatile("" : "+v"(vp[k][b]));
  }

  {
    u32x4 qa[4] = {};
    const unsigned off = W_Q + (unsigned)((g * 8 + (t >> 5)) * 128 + (t & 31)) * 16;
    bool ok[4] = {false, false, false, false};
    for (unsigned spins = 0; spins < SPIN_CAP; ++spins) {
      asm volatile("" ::: "memory");
#pragma unroll
      for (int i = 0; i < 4; ++i) poll_gran(ws, off + i * 512, ok[i], qa[i]);
#pragma unroll
      for (int i = 0; i < 4; ++i) ok[i] = qa[i][1] == tlo && qa[i][3] == thi;
      if (__all(ok[0] && ok[1] && ok[2] && ok[3])) break;
      poll_pause(!(ok[0] && ok[1] && ok[2] && ok[3]));
    }
    const float q0 = (__uint_as_float(qa[0][0]) + __uint_as_float(qa[1][0])) + (__uint_as_float(qa[2][0]) + __uint_as_float(qa[3][0]));
    const float q1 = (__uint_as_float(qa[0][2]) + __uint_as_float(qa[1][2])) + (__uint_as_float(qa[2][2]) + __uint_as_float(qa[3][2]));
    *(LAS unsigned*)(L + L_Q + 4 * t) = pack2(q0, q1);
    asm volatile("s_waitcnt lgkmcnt(0)" ::: "memory");
    if (lane == 0) __hip_atomic_fetch_add((LAS unsigned*)(ctrl + 9), 1u, __ATOMIC_RELAXED, __HIP_MEMORY_SCOPE_WORKGROUP);
    for (unsigned spins = 0; LDSU(ctrl[9]) < 4u && spins < SPIN_CAP; ++spins) {}
  }
  asm volatile("" ::: "memory");
  STAMP(3);
  bf16x8 qf[4];
  {
    const int col = lane & 15, gg = lane >> 4;
#pragma unroll
    for (int m = 0; m < 4; ++m) qf[m] = *(const LAS bf16x8*)(L + L_Q + (col & 3) * 256 + 64 * m + 16 * gg);
  }

  constexpr float CS = 0.08838834764831845f * 1.4426950408889634f;
  f32x4 acc[8], accl = {0.f, 0.f, 0.f, 0.f};
#pragma unroll
  for (int b = 0; b < 8; ++b) acc[b] = f32x4{0.f, 0.f, 0.f, 0.f};
  float mrun = -INFINITY, mc = 0.f;
  const s16x4 ones = {0x3F80, 0x3F80, 0x3F80, 0x3F80};
  auto scores = [&](const bf16x8 (&kf)[4]) -> s16x4 {
    f32x4 sc = {0.f, 0.f, 0.f, 0.f};
#pragma unroll
    for (int m = 0; m < 4; ++m) sc = __builtin_amdgcn_mfma_f32_16x16x32_bf16(kf[m], qf[m], sc, 0, 0, 0);
    const float mx = fmaxf(fmaxf(sc[0], sc[1]), fmaxf(sc[2], sc[3]));
    if (__any(mx > mrun)) {
      const float mn = fmaxf(mrun, xrow_max(mx));
      const float alpha = __builtin_amdgcn_exp2f((mrun - mn) * CS);
      mrun = mn; mc = mn * CS;
#pragma unroll
      for (int b = 0; b < 8; ++b) acc[b] *= alpha;
      accl *= alpha;
    }
    const float p0 = __builtin_amdgcn_exp2f(fmaf(sc[0], CS, -mc)), p1 = __builtin_amdgcn_exp2f(fmaf(sc[1], CS, -mc));
    const float p2 = __builtin_amdgcn_exp2f(fmaf(sc[2], CS, -mc)), p3 = __builtin_amdgcn_exp2f(fmaf(sc[3], CS, -mc));
    const u32x2 pp = {pack2(p0, p1), pack2(p2, p3)};
    return __builtin_bit_cast(s16x4, pp);
  };
  auto pv = [&](const bf16x4 (&vf)[8], s16x4 pf) {
    accl = __builtin_amdgcn_mfma_f32_16x16x16bf16_1k(ones, pf, accl, 0, 0, 0);
#pragma unroll
    for (int b = 0; b < 8; ++b) acc[b] = __builtin_amdgcn_mfma_f32_16x16x16bf16_1k(__builtin_bit_cast(s16x4, vf[b]), pf, acc[b], 0, 0, 0);
  };
  STAMP(4);
#pragma unroll
  for (int k = 0; k < NPRE; ++k) pv(vp[k], scores(kp[k]));
  const int kw = ((nstat - cw + 3) >> 2) - 1;
  for (int k = NPRE; k < kw; ++k) {
    bf16x8 kf[4];
    bf16x4 vf[8];
    (void)await(k);
    fetch(kf, vf);
    const s16x4 pf = scores(kf);
    release(k);
    pv(vf, pf);
  }
  for (unsigned spins = 0; LDSU(ctrl[1]) < (has_job ? 20u : 4u) && spins < SPIN_CAP; ++spins) __builtin_amdgcn_s_sleep(1);
  asm volatile("" ::: "memory");
  f32x2 wof[16];
#pragma unroll
  for (int r = 0; r < 16; ++r) {
    const unsigned u = *(const LAS unsigned*)(L + L_WO + r * 1024 + 4 * t);
    wof[r] = f32x2{bflo(u), bfhi(u)};
  }
#pragma unroll
  for (int r = 0; r < 16; ++r) asm volatile("" : "+v"(wof[r]));
  unsigned o1off = W_O + (unsigned)((((g * 4 + cw) * 8 + (lane >> 3)) * 32 + s) * 8 + (lane & 7)) * 16;
  unsigned ooff = W_O + (unsigned)(((g * 4 + (s >> 3)) * 8 + (s & 7)) * 256 + t) * 16;
  unsigned soff_out = W_S + (unsigned)(((t / GPF) * 256 + c) * GPF + t % GPF) * 16;
  unsigned mgw = (unsigned)(size_t)(L + L_MRG) + 4 * ((cw * 4 + (lane & 3)) * 128 + 32 * ((lane >> 2) & 3) + 4 * (lane >> 4));
  unsigned mgr = (unsigned)(size_t)(L + L_MRG) + 4 * (cw * 128 + 2 * lane);
  asm volatile("" : "+v"(o1off), "+v"(ooff), "+v"(soff_out), "+v"(mgw), "+v"(mgr));
  for (int k = kw; await(k); ++k) {
    bf16x8 kf[4];
    bf16x4 vf[8];
    fetch(kf, vf);
    const s16x4 pf = scores(kf);
    release(k);
    pv(vf, pf);
  }
  STAMP(5);

  {
    const int rep = (lane >> 2) & 3;
    const f32x4 va0 = rep == 0 ? acc[0] : rep == 1 ? acc[2] : rep == 2 ? acc[4] : acc[6];
    const f32x4 va1 = rep == 0 ? acc[1] : rep == 1 ? acc[3] : rep == 2 ? acc[5] : acc[7];
    *(LAS f32x4*)(size_t)(mgw) = va0;
    *(LAS f32x4*)(size_t)(mgw + 64) = va1;
    if (lane < 4) *(LAS f32x2*)(L + L_ML + (lane * 4 + cw) * 8) = f32x2{mc, accl[0]};
  }
  asm volatile("s_waitcnt lgkmcnt(0)" ::: "memory");
  if (lane == 0) __hip_atomic_fetch_add((LAS unsigned*)(ctrl + 12), 1u, __ATOMIC_RELAXED, __HIP_MEMORY_SCOPE_WORKGROUP);
  for (unsigned spins = 0; LDSU(ctrl[12]) < 4u && spins < SPIN_CAP; ++spins) {}
  asm volatile("" ::: "memory");
  {
    const unsigned tb = thi & 0xffff0000u;
    const int head = cw;
    const f32x4 m01 = *(const LAS f32x4*)(L + L_ML + head * 32), m23 = *(const LAS f32x4*)(L + L_ML + head * 32 + 16);
    const f32x2 o0 = *(const LAS f32x2*)(size_t)(mgr), o1 = *(const LAS f32x2*)(size_t)(mgr + 2048);
    const f32x2 o2 = *(const LAS f32x2*)(size_t)(mgr + 4096), o3 = *(const LAS f32x2*)(size_t)(mgr + 6144);
    const float M = fmaxf(fmaxf(m01[0], m01[2]), fmaxf(m23[0], m23[2]));
    const float f0 = __builtin_amdgcn_exp2f(m01[0] - M), f1 = __builtin_amdgcn_exp2f(m01[2] - M);
    const float f2 = __builtin_amdgcn_exp2f(m23[0] - M), f3 = __builtin_amdgcn_exp2f(m23[2] - M);
    const float Ls = fmaf(f0, m01[1], f1 * m01[3]) + fmaf(f2, m23[1], f3 * m23[3]);
    const float rl = __builtin_amdgcn_rcpf(Ls);
    const f32x2 o = (f0 * o0 + f1 * o1 + (f2 * o2 + f3 * o3)) * rl;
    const unsigned lam = __float_as_uint(M + __builtin_amdgcn_logf(Ls));
    st_gran(ws, o1off, __float_as_uint(o[0]), tlo, __float_as_uint(o[1]), tb | ((lane & 1) ? (lam & 0xffffu) : (lam >> 16)));
  }
  STAMP(6);

  u32x4 sg[GPF] = {};
  const unsigned soff = W_S + (unsigned)((fin * 256 + t / GPF) * GPF + t % GPF) * 16;

  {
    const unsigned tb = thi & 0xffff0000u;
    u32x4 og = {};
    bool ok = false;
    for (unsigned spins = 0; spins < SPIN_CAP; ++spins) {
      asm volatile("" ::: "memory");
      poll_gran(ws, ooff, ok, og);
      ok = og[1] == tlo && (og[3] & 0xffff0000u) == tb;
      if (__all(ok)) break;
      poll_pause(!ok);
    }
    STAMP(7);
    if (is_fin) {
#pragma unroll
      for (int i = 0; i < GPF; ++i) sg[i] = ld_gran(ws, soff + i * 4096);
    }
    const unsigned mine = og[3] & 0xffffu, other = dppu<XOR1>(mine);
    const float lam = __uint_as_float((lane & 1) ? ((other << 16) | mine) : ((mine << 16) | other));
    const float Mw = max_mod8(lam);
    const float wt = __builtin_amdgcn_exp2f(lam - Mw);
    const float a0 = sum_mod8(wt * __uint_as_float(og[0])), a1 = sum_mod8(wt * __uint_as_float(og[2])), Ww = sum_mod8(wt);
    LAS float* r2 = (LAS float*)(L + L_RED);
    if (lane < 8) { r2[(2 * lane) * 4 + cw] = a0; r2[(2 * lane + 1) * 4 + cw] = a1; }
    if (lane == 8) { r2[64 + cw] = Mw; r2[68 + cw] = Ww; }
    asm volatile("s_waitcnt lgkmcnt(0)" ::: "memory");
    __builtin_amdgcn_s_barrier();
    const f32x4 ov = *(const LAS f32x4*)(r2 + 4 * (lane & 15)), mv = *(const LAS f32x4*)(r2 + 64), wv = *(const LAS f32x4*)(r2 + 68);
    const float M = fmaxf(fmaxf(mv[0], mv[1]), fmaxf(mv[2], mv[3]));
    const float f0 = __builtin_amdgcn_exp2f(mv[0] - M), f1 = __builtin_amdgcn_exp2f(mv[1] - M);
    const float f2 = __builtin_amdgcn_exp2f(mv[2] - M), f3 = __builtin_amdgcn_exp2f(mv[3] - M);
    const float num = fmaf(f0, ov[0], f1 * ov[1]) + fmaf(f2, ov[2], f3 * ov[3]);
    const float den = fmaf(f0, wv[0], f1 * wv[1]) + fmaf(f2, wv[2], f3 * wv[3]);
    const float od = rbf(num * __builtin_amdgcn_rcpf(den));
    f32x2 sa = {0.f, 0.f}, sb = {0.f, 0.f};
#pragma unroll
    for (int r = 0; r < 16; r += 2) {
      const float oa = __builtin_bit_cast(float, __builtin_amdgcn_readlane(__builtin_bit_cast(int, od), r));
      const float ob = __builtin_bit_cast(float, __builtin_amdgcn_readlane(__builtin_bit_cast(int, od), r + 1));
      sa += oa * wof[r]; sb += ob * wof[r + 1];
    }
    const f32x2 sv = sa + sb;
    st_gran(ws, soff_out, __float_as_uint(sv[0]), tlo, __float_as_uint(sv[1]), thi);
  }
  STAMP(8);

  if (is_fin) {
    bool sok[GPF];
#pragma unroll
    for (int i = 0; i < GPF; ++i) sok[i] = sg[i][1] == tlo && sg[i][3] == thi;
    for (unsigned spins = 0; spins < SPIN_CAP; ++spins) {
      bool all = true;
#pragma unroll
      for (int i = 0; i < GPF; ++i) all = all && sok[i];
      if (__all(all)) break;
      if (spins) poll_pause(!all);
      asm volatile("" ::: "memory");
#pragma unroll
      for (int i = 0; i < GPF; ++i) poll_gran(ws, soff + i * 4096, sok[i], sg[i]);
#pragma unroll
      for (int i = 0; i < GPF; ++i) sok[i] = sg[i][1] == tlo && sg[i][3] == thi;
    }
    STAMP(9);
    float a0 = 0.f, a1 = 0.f;
#pragma unroll
    for (int i = 0; i < GPF; i += 2) {
      a0 += __uint_as_float(sg[i][0]) + __uint_as_float(sg[i + 1][0]);
      a1 += __uint_as_float(sg[i][2]) + __uint_as_float(sg[i + 1][2]);
    }
    a0 = GPF == 4 ? sum_mod4(a0) : sum_mod8(a0); a1 = GPF == 4 ? sum_mod4(a1) : sum_mod8(a1);
    LAS float* r3 = (LAS float*)(L + L_RED) + 96;
    if (lane < GPF) { r3[(2 * lane) * 4 + cw] = a0; r3[(2 * lane + 1) * 4 + cw] = a1; }
    asm volatile("s_waitcnt lgkmcnt(0)" ::: "memory");
    __builtin_amdgcn_s_barrier();
    if (t < GPF) {
      const f32x4 u0 = *(const LAS f32x4*)(r3 + 8 * t), u1 = *(const LAS f32x4*)(r3 + 8 * t + 4);
      const f32x2 gv = *(const LAS f32x2*)(L + L_RED + 768 + 8 * t);
      const float s0 = rbf((u0[0] + u0[1]) + (u0[2] + u0[3])), s1 = rbf((u1[0] + u1[1]) + (u1[2] + u1[3]));
      unsigned res = pack2(bflo(xo) + rbf(s0 * gv[0]), bfhi(xo) + rbf(s1 * gv[1]));
      *(GAS unsigned*)(out + 2 * GPF * fin + 2 * t) = res;
      if (fin == 0 && t == 0) __builtin_amdgcn_raw_buffer_store_b64(u32x2{tlo, thi}, ws, W_EPOCH, 0, 0);
    }
    STAMP(10);
  }
  STAMP_FLUSH();
}

}

extern "C" void kernel_launch(void* const* d_in, const int* in_sizes, int n_in, void* d_out, int out_size, void* d_ws, size_t ws_size,
                              hipStream_t stream) {
  (void)in_sizes; (void)n_in; (void)out_size; (void)ws_size;
  (void)hipFuncSetAttribute((const void*)fused_decode_kernel, hipFuncAttributeMaxDynamicSharedMemorySize, L_END);
  hipLaunchKernelGGL(fused_decode_kernel, dim3(NWG), dim3(NTHR), L_END, stream,
                     (const unsigned short*)d_in[0], (const unsigned short*)d_in[1], (const unsigned short*)d_in[2],
                     (const unsigned short*)d_in[3], (const unsigned short*)d_in[4], (const unsigned short*)d_in[6],
                     (const unsigned short*)d_in[7], (unsigned short*)d_out, (unsigned char*)d_ws);
}
```

```cpp
#include <hip/hip_runtime.h>
#include <hip/hip_bf16.h>
#include <stdint.h>

#define LAS __attribute__((address_space(3)))
#define GAS __attribute__((address_space(1)))
typedef unsigned u32x4 __attribute__((ext_vector_type(4)));
typedef unsigned u32x2 __attribute__((ext_vector_type(2)));
typedef int i32x4 __attribute__((ext_vector_type(4)));
typedef float f32x4 __attribute__((ext_vector_type(4)));
typedef float f32x2 __attribute__((ext_vector_type(2)));
typedef __bf16 bf16x8 __attribute__((ext_vector_type(8)));
typedef __bf16 bf16x4 __attribute__((ext_vector_type(4)));
typedef __bf16 bf16x2 __attribute__((ext_vector_type(2)));
typedef short s16x4 __attribute__((ext_vector_type(4)));

namespace {

constexpr int DM = 512, HKV = 8, SK = 65536, DH = 128, DFF = 1792;
constexpr int NWG = 256, NTHR = 384;
#define MAP_SPREAD 1
#define POOLPER 14
#define XBACK 26
#define XPOLL 10
#define SIDE_TAU 32
#define TAU 28
constexpr int POOL = 32 * POOLPER;
#define FFN_SHED 14
#define RING_ 16
constexpr int RING = RING_, SLOT = 8192;
#define INFL_ 3
#define KPOL "nt"
#define VPOL "nt"
#define WPOL "nt"
#define BACK_HI 32
#define BACK_LO 12
#define BACK_SLEEP_HI 4
#define BACK_SLEEP_LO 2
#define AUX_PRIO 3
#define CSPIN_SLEEP 0
#define AUX_SLEEP 8
#define AUX_LEAD 0
#define NBURST 8
#define SIDE2_FIRST 24
#define AUX_FIRST 12
#define AUX_STEP 6
#define WAITN_ (8 * (INFL_ - 1))
constexpr int WAITN = WAITN_;
constexpr int INFL = (WAITN + 7) / 8 + 1;
constexpr int NJOB = DFF / 64;
#define GPF_ 4
constexpr int GPF = GPF_;
constexpr int NFIN = 256 / GPF;
constexpr int FPG = NFIN / 8;
constexpr unsigned KV_BYTES = (unsigned)HKV * SK * DH * 2;

constexpr int L_RING = 0;
constexpr int L_WO = RING * SLOT;
constexpr int L_CTRL = L_WO + 16384;
constexpr int L_XN = L_CTRL + 256;
constexpr int L_Q = L_XN + 2048;
constexpr int L_RED = L_Q + 1024;
constexpr int L_FT = L_RED + 1024;
constexpr int L_MRG = L_FT + 2048;
constexpr int L_ML = L_MRG + 8192;
constexpr int L_END = L_ML + 256;
static_assert(L_END <= 160 * 1024, "LDS");

constexpr unsigned W_EPOCH = 0;
constexpr unsigned W_Q = 4096;
constexpr unsigned W_O = W_Q + 8 * 8 * 4 * 32 * 16;
constexpr unsigned W_S = W_O + 8 * 4 * 8 * 32 * 8 * 16;
constexpr unsigned W_F = W_S + 256 * 256 * 16;
constexpr unsigned W_T = W_F + NJOB * 256 * 16;
constexpr unsigned W_END = W_T + 8 * 32 * 16;

constexpr unsigned SPIN_CAP = 1u << 20;
#define LDSU(x) ((unsigned)__builtin_amdgcn_readfirstlane(x))
#define NPRE_ 3
constexpr int NPRE = NPRE_;
#define LDELAY 0
#define DELAY1 32
#define DELAY2 32
#define EARLY_S 0
#define POLL_SLEEP 0

#define STAMP(i) do {} while (0)
#define STAMP_FLUSH() do {} while (0)

__device__ __forceinline__ float bf2f(unsigned short b) { return __builtin_bit_cast(float, (unsigned)b << 16); }
__device__ __forceinline__ float bflo(unsigned u) { return __builtin_bit_cast(float, u << 16); }
__device__ __forceinline__ float bfhi(unsigned u) { return __builtin_bit_cast(float, u & 0xffff0000u); }
__device__ __forceinline__ unsigned short f2bf(float f) { return __builtin_bit_cast(unsigned short, (__bf16)f); }
__device__ __forceinline__ float rbf(float f) { return bf2f(f2bf(f)); }
__device__ __forceinline__ unsigned pack2(float lo, float hi) {
  const f32x2 v = {lo, hi};
  return __builtin_bit_cast(unsigned, __builtin_convertvector(v, bf16x2));
}

template <int CTRL> __device__ __forceinline__ float dpp(float x) {
  return __builtin_bit_cast(float, __builtin_amdgcn_mov_dpp(__builtin_bit_cast(int, x), CTRL, 0xf, 0xf, true));
}
template <int CTRL> __device__ __forceinline__ unsigned dppu(unsigned x) {
  return (unsigned)__builtin_amdgcn_mov_dpp((int)x, CTRL, 0xf, 0xf, true);
}
constexpr int XOR1 = 0xB1, XOR2 = 0x4E, ROR4 = 0x124, ROR8 = 0x128;
__device__ __forceinline__ float xrow_sum(float x) {
  auto s = __builtin_amdgcn_permlane16_swap(__float_as_uint(x), __float_as_uint(x), false, false);
  x = __uint_as_float(s[0]) + __uint_as_float(s[1]);
  auto t = __builtin_amdgcn_permlane32_swap(__float_as_uint(x), __float_as_uint(x), false, false);
  return __uint_as_float(t[0]) + __uint_as_float(t[1]);
}
__device__ __forceinline__ float xrow_max(float x) {
  auto s = __builtin_amdgcn_permlane16_swap(__float_as_uint(x), __float_as_uint(x), false, false);
  x = fmaxf(__uint_as_float(s[0]), __uint_as_float(s[1]));
  auto t = __builtin_amdgcn_permlane32_swap(__float_as_uint(x), __float_as_uint(x), false, false);
  return fmaxf(__uint_as_float(t[0]), __uint_as_float(t[1]));
}
__device__ __forceinline__ float wave_sum(float x) {
  x += dpp<XOR1>(x); x += dpp<XOR2>(x); x += dpp<ROR4>(x); x += dpp<ROR8>(x);
  return xrow_sum(x);
}
__device__ __forceinline__ float sum_mod2(float x) {
  x += dpp<XOR2>(x); x += dpp<ROR4>(x); x += dpp<ROR8>(x);
  return xrow_sum(x);
}
__device__ __forceinline__ float sum_mod4(float x) {
  x += dpp<ROR4>(x); x += dpp<ROR8>(x);
  return xrow_sum(x);
}
__device__ __forceinline__ float sum_mod8(float x) { x += dpp<ROR8>(x); return xrow_sum(x); }
__device__ __forceinline__ float max_mod8(float x) { x = fmaxf(x, dpp<ROR8>(x)); return xrow_max(x); }

__device__ __forceinline__ u32x4 ld_nt16(const void* p) { return __builtin_nontemporal_load((const GAS u32x4*)p); }

__device__ __forceinline__ void poll_pause(bool missing) {
  const int n = __builtin_popcountll(__ballot(missing));
  if (n > BACK_HI) __builtin_amdgcn_s_sleep(BACK_SLEEP_HI);
  else if (n > BACK_LO) __builtin_amdgcn_s_sleep(BACK_SLEEP_LO);
  else if (POLL_SLEEP) __builtin_amdgcn_s_sleep(POLL_SLEEP);
}

__device__ __forceinline__ void st_gran(__amdgpu_buffer_rsrc_t ws, unsigned off, unsigned a, unsigned b, unsigned c, unsigned d) {
  const u32x4 g = {a, b, c, d};
  __builtin_amdgcn_raw_buffer_store_b128(g, ws, off, 0, 16);
}
__device__ __forceinline__ u32x4 ld_gran(__amdgpu_buffer_rsrc_t ws, unsigned off) {
  return __builtin_amdgcn_raw_buffer_load_b128(ws, off, 0, 16);
}
__device__ __forceinline__ void poll_gran(__amdgpu_buffer_rsrc_t ws, unsigned off, bool have, u32x4& g) {
  const u32x4 v = __builtin_amdgcn_raw_buffer_load_b128(ws, have ? 0x7ffffff0u : off, 0, 16);
#pragma unroll
  for (int k = 0; k < 4; ++k) g[k] = have ? g[k] : v[k];
}

__device__ __forceinline__ int job_of(int g, int s) { return g * 4 + (s >> 3); }

__global__ void __launch_bounds__(NTHR, 2)
fused_decode_kernel(const unsigned short* __restrict__ x, const unsigned short* __restrict__ kc, const unsigned short* __restrict__ vc,
                    const unsigned short* __restrict__ wq, const unsigned short* __restrict__ wo, const unsigned short* __restrict__ w1,
                    const unsigned short* __restrict__ w2, unsigned short* __restrict__ out, unsigned char* __restrict__ wsp) {
  extern __shared__ __attribute__((aligned(16))) unsigned char lds[];
  LAS unsigned char* L = (LAS unsigned char*)lds;
  volatile LAS unsigned* ctrl = (volatile LAS unsigned*)(L + L_CTRL);
  const int w = blockIdx.x;
  const int g = w >> 5, s = 4 * (w & 7) + ((w >> 3) & 3);
  const int wave = __builtin_amdgcn_readfirstlane(threadIdx.x >> 6), lane = threadIdx.x & 63;
  constexpr int NF_J = 128 - FFN_SHED, NF_O = 128 + FFN_SHED / 7;
  const bool has_job = g < 7 && (s & 7) == 2;
  auto first_fill = [&](int i) -> int { return g == 7 ? 128 * i : NF_O * i - (NF_O - NF_J) * ((i + 5) >> 3); };
  const int fill0 = first_fill(s);
  const int nstat = (g == 7 ? 128 : has_job ? NF_J : NF_O) - POOLPER;
  STAMP(0);

  if (wave == 0) {
    const unsigned long long kb = (unsigned long long)kc, vb = (unsigned long long)vc;
    const i32x4 sk = {(int)kb, (int)(kb >> 32), (int)KV_BYTES, 0x00020000};
    const i32x4 sv = {(int)vb, (int)(vb >> 32), (int)KV_BYTES, 0x00020000};
    const unsigned base = (unsigned)g * (SK * DH * 2) + (unsigned)fill0 * 4096;
    const unsigned r4 = lane >> 4, p = lane & 15;
    const unsigned k0 = r4 * 256 + 16 * (p ^ (0 + r4)), k1 = r4 * 256 + 16 * (p ^ (4 + r4));
    const unsigned k2 = r4 * 256 + 16 * (p ^ (8 + r4)), k3 = r4 * 256 + 16 * (p ^ (12 + r4));
    const unsigned v0 = r4 * 256 + 16 * (p ^ (r4 << 1)), v1 = r4 * 256 + 16 * (p ^ ((4 + r4) << 1));
    const unsigned ring0 = (unsigned)(size_t)(L + L_RING);
    __builtin_amdgcn_s_setprio(3);
    const int naux = has_job ? 20 : 4, job = job_of(g, s);
    const unsigned vq = (lane >> 3) * 8192 + (lane & 7) * 16, v1w = (lane >> 3) * (DFF * 2) + (lane & 7) * 16, vc16 = lane * 16;
    const unsigned aux0 = (unsigned)(size_t)(L + L_WO);
    const unsigned long long qb = (unsigned long long)wq, ob = (unsigned long long)wo, b1 = (unsigned long long)w1, b2 = (unsigned long long)w2;
    const i32x4 sq = {(int)qb, (int)(qb >> 32), DM * 4096 * 2, 0x00020000}, so = {(int)ob, (int)(ob >> 32), 4096 * DM * 2, 0x00020000};
    const i32x4 s1 = {(int)b1, (int)(b1 >> 32), DM * DFF * 2, 0x00020000}, s2 = {(int)b2, (int)(b2 >> 32), DM * DFF * 2, 0x00020000};
    const __amdgpu_buffer_rsrc_t wsr = __builtin_amdgcn_make_buffer_rsrc((void*)wsp, 0, (int)W_END, 0x00020000);
    unsigned long long epl;
    asm volatile("global_load_dwordx2 %0, %1, %2 sc1" : "=v"(epl) : "v"(0u), "s"(wsp) : "memory");
    if (lane < 16) ctrl[lane] = 0u;
    asm volatile("s_waitcnt lgkmcnt(0)" ::: "memory");
    __builtin_amdgcn_s_barrier();
    if (LDELAY) __builtin_amdgcn_s_sleep(LDELAY);
#define PARK_BEGIN
#define PARK_END
#define PROGRESS
#define KV_ISSUE(WAIT) asm volatile( \
            WAIT "s_mov_b32 m0, %0\n\ts_nop 0\n\t" \
            "buffer_load_dwordx4 %2, %8, %10 offen " KPOL " lds\n\t" \
            "buffer_load_dwordx4 %3, %8, %10 offen offset:1024 " KPOL " lds\n\t" \
            "buffer_load_dwordx4 %4, %8, %10 offen offset:2048 " KPOL " lds\n\t" \
            "buffer_load_dwordx4 %5, %8, %10 offen offset:3072 " KPOL " lds\n\t" \
            "s_mov_b32 m0, %1\n\ts_nop 0\n\t" \
            "buffer_load_dwordx4 %6, %9, %10 offen " VPOL " lds\n\t" \
            "buffer_load_dwordx4 %7, %9, %10 offen offset:1024 " VPOL " lds\n\t" \
            "buffer_load_dwordx4 %6, %9, %10 offen offset:2048 " VPOL " lds\n\t" \
            "buffer_load_dwordx4 %7, %9, %10 offen offset:3072 " VPOL " lds" \
            :: "s"(m0k), "s"(m0k + 4096), "v"(k0), "v"(k1), "v"(k2), "v"(k3), "v"(v0), "v"(v1), "s"(sk), "s"(sv), "s"(sko), \
               "i"(WAITN) : "memory")
#define SIDE_ISSUE(SRD, VO, SO0, RSTEP) do { \
          const unsigned so0_ = (SO0), st_ = (RSTEP) - 1024, so4_ = so0_ + 4 * (RSTEP); \
          asm volatile( \
            "s_waitcnt vmcnt(%12)\n\t" \
            "s_mov_b32 m0, %0\n\ts_nop 0\n\t" \
            "buffer_load_dwordx4 %2, %3, %4 offen " WPOL " lds\n\t" \
            "buffer_load_dwordx4 %2, %3, %5 offen offset:1024 " WPOL " lds\n\t" \
            "buffer_load_dwordx4 %2, %3, %6 offen offset:2048 " WPOL " lds\n\t" \
            "buffer_load_dwordx4 %2, %3, %7 offen offset:3072 " WPOL " lds\n\t" \
            "s_mov_b32 m0, %1\n\ts_nop 0\n\t" \
            "buffer_load_dwordx4 %2, %3, %8 offen " WPOL " lds\n\t" \
            "buffer_load_dwordx4 %2, %3, %9 offen offset:1024 " WPOL " lds\n\t" \
            "buffer_load_dwordx4 %2, %3, %10 offen offset:2048 " WPOL " lds\n\t" \
            "buffer_load_dwordx4 %2, %3, %11 offen offset:3072 " WPOL " lds" \
            :: "s"(dst), "s"(dst + 4096), "v"(VO), "s"(SRD), "s"(so0_), "s"(so0_ + st_), "s"(so0_ + 2 * st_), "s"(so0_ + 3 * st_), \
               "s"(so4_), "s"(so4_ + st_), "s"(so4_ + 2 * st_), "s"(so4_ + 3 * st_), "i"(WAITN) : "memory"); } while (0)
    auto side_pos = [&](int a) -> int { return a >= naux ? 0x7fffffff : a < 2 ? a : has_job ? AUX_FIRST + AUX_STEP * (a - 2) : SIDE2_FIRST + 6 * (a - 2); };
    unsigned m0k = ring0, sko = base;
#pragma unroll
    for (int a = 0; a < 2; ++a) {
      const unsigned dst = aux0 + (unsigned)a * 8192;
      SIDE_ISSUE(sq, vq, (unsigned)(128 * (s >> 3) + 64 * a) * 8192 + g * 1024 + (s & 7) * 128, 8 * 8192u);
    }
#pragma unroll
    for (int j = 0; j < NBURST - 2; ++j) { KV_ISSUE(""); m0k += SLOT; sko += 4096; }
    asm volatile("s_waitcnt vmcnt(%0)" :: "i"(8 * (NBURST - 2)) : "memory");
    *(volatile LAS u32x2*)(L + L_CTRL) = u32x2{0u, 2u};
    asm volatile("" : "+v"(epl));
    const unsigned long long tagl = epl + 1ull;
    const unsigned tlo = __builtin_amdgcn_readfirstlane((unsigned)tagl), thi = __builtin_amdgcn_readfirstlane((unsigned)(tagl >> 32));
    int kv = NBURST - 2, ax = 2, e = NBURST, le = 2, axl = 2, lim = RING, alim = 2;
    int nextpos = side_pos(2), nextland = nextpos;
    const int nent = nstat + naux;
    const unsigned ring_end = ring0 + RING * SLOT;
    u32x4 dn = {0u, 0u, 0u, 0u};
    unsigned da = 0u;
#define ACCOUNT() do { \
        le = max(le, e + 1 - INFL); \
        if (le > nextland) { ++axl; nextland = side_pos(axl); } \
        *(volatile LAS u32x2*)(L + L_CTRL) = u32x2{(unsigned)(le - axl), (unsigned)axl}; \
        lim = max(lim, RING + 4 * (int)LDSU(min(min(dn[0], dn[1]), min(dn[2], dn[3])))); \
        alim = max(alim, 2 + (int)LDSU(da)); \
        dn = *(volatile LAS u32x4*)(L + L_CTRL + 16); \
        da = ctrl[3]; \
      } while (0)
#define PARK(COND) do { \
        asm volatile("s_waitcnt vmcnt(0)" ::: "memory"); \
        le = e; while (le > nextland) { ++axl; nextland = side_pos(axl); } \
        *(volatile LAS u32x2*)(L + L_CTRL) = u32x2{(unsigned)(le - axl), (unsigned)axl}; \
        PARK_BEGIN \
        for (unsigned spins = 0; spins < SPIN_CAP; ++spins) { if (COND) break; __builtin_amdgcn_s_sleep(1); } \
        PARK_END \
        asm volatile("" ::: "memory"); \
      } while (0)
#define KV_STEP() do { \
        if (kv >= lim) { PARK(LDSU(ctrl[4 + ((kv - RING) & 3)]) > (unsigned)((kv - RING) >> 2)); lim = kv + 1; } \
        KV_ISSUE("s_waitcnt vmcnt(%11)\n\t"); \
        m0k += SLOT; if (m0k == ring_end) m0k = ring0; \
        sko += 4096; \
        ++kv; \
        ACCOUNT(); \
        PROGRESS \
      } while (0)
    bool stamped = false;
    while (e < nent) {
      const int stop = min(nextpos, nent);
      for (; e < stop; ++e) {
        KV_STEP();
        if (kv == nstat - XBACK) {
          const unsigned now = (unsigned)__builtin_amdgcn_s_memrealtime() + (unsigned)((naux - ax) * SIDE_TAU);
          if (lane == 0) st_gran(wsr, W_T + (unsigned)(g * 32 + s) * 16, now, tlo, 0u, thi);
          stamped = true;
        }
      }
      if (e < nent) {
        if (ax >= alim) { PARK(LDSU(ctrl[3]) + 2u > (unsigned)ax); alim = ax + 1; }
        const unsigned dst = aux0 + (unsigned)(ax & 1) * 8192;
        if (ax >= naux - 2) SIDE_ISSUE(so, vc16, (unsigned)(16 * (g * 32 + s) + 8 * (ax - (naux - 2))) * 1024, 1024u);
        else if (ax < 10) SIDE_ISSUE(s1, v1w, (unsigned)(64 * (ax - 2)) * (DFF * 2) + job * 128, 8u * DFF * 2);
        else SIDE_ISSUE(s2, vc16, (unsigned)(64 * job + 8 * (ax - 10)) * 1024, 1024u);
        ++ax;
        nextpos = side_pos(ax);
        ACCOUNT();
        ++e;
      }
    }
    (void)stamped;
    if (LDSU(ctrl[13]) == 0u) PARK(LDSU(ctrl[13]) != 0u);
    const int share = (int)LDSU(ctrl[14]);
    ctrl[2] = (unsigned)(kv + share);
    for (int tix = (int)LDSU(ctrl[13]) - 1, stop = tix + share; tix < stop; ++tix, ++e) {
      const int gap = tix / POOLPER;
      sko = (unsigned)g * (SK * DH * 2) + (unsigned)(first_fill(gap + 1) - POOLPER + (tix - gap * POOLPER)) * 4096;
      KV_STEP();
    }
#pragma unroll
    for (int r = INFL - 1; r >= 0; --r) {
      asm volatile("s_waitcnt vmcnt(%0)" :: "i"(8 * r) : "memory");
      le = max(le, e - r);
      while (le > nextland) { ++axl; nextland = side_pos(axl); }
      *(volatile LAS u32x2*)(L + L_CTRL) = u32x2{(unsigned)(le - axl), (unsigned)axl};
    }
#undef KV_STEP
#undef KV_ISSUE
#undef SIDE_ISSUE
#undef ACCOUNT
#undef PARK
    STAMP(2);
    STAMP_FLUSH();
    return;
  }

  const __amdgpu_buffer_rsrc_t ws = __builtin_amdgcn_make_buffer_rsrc((void*)wsp, 0, (int)W_END, 0x00020000);
  const int c = g * 32 + s;
  const bool is_fin = s % (32 / FPG) == 1;
  const int fin = g * FPG + s / (32 / FPG);

  if (wave == 5) {
    const u32x2 ep = __builtin_amdgcn_raw_buffer_load_b64(ws, W_EPOCH, 0, 16);
    unsigned touch;
    {
      const unsigned char* p = (const unsigned char*)x;
      if (lane == 1) p = (const unsigned char*)wo + (size_t)c * 16384;
      if (lane == 2) p = (const unsigned char*)w1 + (size_t)(has_job ? job_of(g, s) * 128 : 0);
      if (lane == 3) p = (const unsigned char*)w1 + (size_t)(DM - 1) * DFF * 2;
      if (lane == 4) p = (const unsigned char*)w2 + (size_t)(has_job ? job_of(g, s) * 65536 : 0);
      if (lane == 5) p = wsp + W_O + (size_t)g * 131072;
      if (lane == 6) p = wsp + W_S;
      if (lane == 7) p = wsp + W_END - 4;
      if (lane == 8) p = (const unsigned char*)out;
      touch = *(const GAS unsigned*)p;
    }
    __builtin_amdgcn_s_barrier();
    asm volatile("" :: "v"(touch));
    for (unsigned spins = 0; LDSU(ctrl[8]) < 4u && spins < SPIN_CAP; ++spins) __builtin_amdgcn_s_sleep(4);
    asm volatile("" ::: "memory");
    const unsigned long long tag = (((unsigned long long)ep[1] << 32) | ep[0]) + 1ull;
    const unsigned tlo = __builtin_amdgcn_readfirstlane((unsigned)tag), thi = __builtin_amdgcn_readfirstlane((unsigned)(tag >> 32));
    if (has_job) {
      const int job = job_of(g, s);
      const int r8 = lane >> 3, cq = lane & 7;
      float h[8];
#pragma unroll
      for (int k = 0; k < 8; ++k) h[k] = 0.f;
#pragma unroll 1
      for (int p = 0; p < 8; ++p) {
        for (unsigned spins = 0; LDSU(ctrl[1]) < (unsigned)(3 + p) && spins < SPIN_CAP; ++spins) __builtin_amdgcn_s_sleep(4);
        asm volatile("" ::: "memory");
        const LAS unsigned char* src = L + L_WO + (p & 1) * 8192 + r8 * 128 + cq * 16;
#pragma unroll
        for (int i = 0; i < 8; ++i) {
          const u32x4 wr = *(const LAS u32x4*)(src + i * 1024);
          const float xn = *(const LAS float*)(L + L_XN + 4 * (64 * p + 8 * i + r8));
#pragma unroll
          for (int k = 0; k < 4; ++k) { h[2 * k] = fmaf(xn, bflo(wr[k]), h[2 * k]); h[2 * k + 1] = fmaf(xn, bfhi(wr[k]), h[2 * k + 1]); }
        }
        asm volatile("s_waitcnt lgkmcnt(0)" ::: "memory");
        if (lane == 0) ctrl[3] = (unsigned)(3 + p);
      }
#pragma unroll
      for (int k = 0; k < 8; ++k) h[k] = fmaxf(rbf(sum_mod8(h[k])), 0.f);
      float bp[8];
#pragma unroll
      for (int k = 0; k < 8; ++k) bp[k] = 0.f;
#pragma unroll 1
      for (int p = 0; p < 8; ++p) {
        for (unsigned spins = 0; LDSU(ctrl[1]) < (unsigned)(11 + p) && spins < SPIN_CAP; ++spins) __builtin_amdgcn_s_sleep(4);
        asm volatile("" ::: "memory");
        const LAS unsigned char* src = L + L_WO + (p & 1) * 8192 + lane * 16;
#pragma unroll
        for (int i = 0; i < 8; ++i) {
          const u32x4 wr = *(const LAS u32x4*)(src + i * 1024);
          const float hv = __builtin_bit_cast(float, __builtin_amdgcn_readlane(__builtin_bit_cast(int, h[i]), p));
#pragma unroll
          for (int k = 0; k < 4; ++k) { bp[2 * k] = fmaf(hv, bflo(wr[k]), bp[2 * k]); bp[2 * k + 1] = fmaf(hv, bfhi(wr[k]), bp[2 * k + 1]); }
        }
        asm volatile("s_waitcnt lgkmcnt(0)" ::: "memory");
        if (lane == 0) ctrl[3] = (unsigned)(11 + p);
      }
      *(LAS f32x4*)(L + L_FT + 32 * lane) = f32x4{bp[0], bp[1], bp[2], bp[3]};
      *(LAS f32x4*)(L + L_FT + 32 * lane + 16) = f32x4{bp[4], bp[5], bp[6], bp[7]};
      asm volatile("s_waitcnt lgkmcnt(0)" ::: "memory");
#pragma unroll
      for (int e = 0; e < 4; ++e) {
        const f32x2 v = *(const LAS f32x2*)(L + L_FT + 8 * (64 * e + lane));
        st_gran(ws, W_F + (unsigned)(job * 256 + 64 * e + lane) * 16, __float_as_uint(v[0]), tlo, __float_as_uint(v[1]), thi);
      }
    }
    {
      for (unsigned spins = 0; (int)LDSU(ctrl[0]) < nstat - XPOLL && spins < SPIN_CAP; ++spins) __builtin_amdgcn_s_sleep(4);
      u32x4 tg = {};
      bool ok = false;
      for (unsigned spins = 0; spins < SPIN_CAP; ++spins) {
        asm volatile("" ::: "memory");
        poll_gran(ws, W_T + (unsigned)(g * 32 + (lane & 31)) * 16, ok, tg);
        ok = tg[1] == tlo && tg[3] == thi;
        if (__all(ok)) break;
        __builtin_amdgcn_s_sleep(24);
      }
      const int d = (int)(tg[0] - (unsigned)__builtin_amdgcn_readlane((int)tg[0], 0));
      const float mean = wave_sum((float)d) * (1.f / 64.f);
      const float a = floorf(fmaxf((float)(POOLPER * TAU) + (mean - (float)d), 0.f));
      float pre = lane < 32 ? a : 0.f;
      pre += __builtin_bit_cast(float, __builtin_amdgcn_update_dpp(0, __builtin_bit_cast(int, pre), 0x111, 0xf, 0xf, true));
      pre += __builtin_bit_cast(float, __builtin_amdgcn_update_dpp(0, __builtin_bit_cast(int, pre), 0x112, 0xf, 0xf, true));
      pre += __builtin_bit_cast(float, __builtin_amdgcn_update_dpp(0, __builtin_bit_cast(int, pre), 0x114, 0xf, 0xf, true));
      pre += __builtin_bit_cast(float, __builtin_amdgcn_update_dpp(0, __builtin_bit_cast(int, pre), 0x118, 0xf, 0xf, true));
      pre += __builtin_bit_cast(float, __builtin_amdgcn_update_dpp(0, __builtin_bit_cast(int, pre), 0x142, 0xa, 0xf, false));
      const float tot = __builtin_bit_cast(float, __builtin_amdgcn_readlane(__builtin_bit_cast(int, pre), 31));
      const float scale = tot > 0.f ? (float)POOL / tot : 0.f;
      const int bnd = lane == 31 ? POOL : min((int)(pre * scale), POOL);
      const int hi = __builtin_amdgcn_readlane(bnd, s), lo = s == 0 ? 0 : __builtin_amdgcn_readlane(bnd, s - 1);
      if (lane == 0) ctrl[14] = (unsigned)(hi - lo);
      asm volatile("s_waitcnt lgkmcnt(0)" ::: "memory");
      if (lane == 0) ctrl[13] = (unsigned)(lo + 1);
    }
    STAMP(1);
    if (is_fin) for (unsigned spins = 0; spins < SPIN_CAP; ++spins) { const u32x4 cv = *(volatile LAS u32x4*)(L + L_CTRL); if (LDSU(cv[2]) != 0u && LDSU(cv[0]) >= LDSU(cv[2])) break; __builtin_amdgcn_s_sleep(AUX_SLEEP); }
    if (is_fin) {
      constexpr int JPL = 64 / GPF, NL = (NJOB + JPL - 1) / JPL;
      u32x4 fg[NL];
      const unsigned oa = W_F + (unsigned)((lane / GPF) * 256 + fin * GPF + (lane % GPF)) * 16;
      for (unsigned spins = 0; spins < SPIN_CAP; ++spins) {
        asm volatile("" ::: "memory");
        bool ok = true;
#pragma unroll
        for (int i = 0; i < NL; ++i) fg[i] = ld_gran(ws, lane / GPF + JPL * i < NJOB ? oa + i * JPL * 256 * 16 : oa);
#pragma unroll
        for (int i = 0; i < NL; ++i) ok = ok && fg[i][1] == tlo && fg[i][3] == thi;
        if (__all(ok)) break;
        __builtin_amdgcn_s_sleep(8);
      }
      float b0 = 0.f, b1 = 0.f;
#pragma unroll
      for (int i = 0; i < NL; ++i) if (lane / GPF + JPL * i < NJOB) { b0 += __uint_as_float(fg[i][0]); b1 += __uint_as_float(fg[i][2]); }
      b0 = rbf(GPF == 4 ? sum_mod4(b0) : sum_mod8(b0)); b1 = rbf(GPF == 4 ? sum_mod4(b1) : sum_mod8(b1));
      const float g0 = rbf(b0 / (1.f + __expf(-b0))), g1 = rbf(b1 / (1.f + __expf(-b1)));
      if (lane < GPF) *(LAS f32x2*)(L + L_RED + 768 + 8 * lane) = f32x2{g0, g1};
      asm volatile("s_waitcnt lgkmcnt(0)" ::: "memory");
    }
    STAMP_FLUSH();
    return;
  }

  const int cw = wave - 1, t = threadIdx.x - 64;

  const u32x2 ep = __builtin_amdgcn_raw_buffer_load_b64(ws, W_EPOCH, 0, 16);
  const u32x4 xr = *(const GAS u32x4*)(x + 8 * lane);
  unsigned xo = *(const GAS unsigned*)(x + 2 * GPF * fin + 2 * (lane % GPF));
  const int trow = 32 * cw + (lane >> 3);
  __builtin_amdgcn_s_barrier();

  const unsigned long long tag = (((unsigned long long)ep[1] << 32) | ep[0]) + 1ull;
  const unsigned tlo = __builtin_amdgcn_readfirstlane((unsigned)tag), thi = __builtin_amdgcn_readfirstlane((unsigned)(tag >> 32));

  {
    float xf[8];
#pragma unroll
    for (int k = 0; k < 4; ++k) { xf[2 * k] = bflo(xr[k]); xf[2 * k + 1] = bfhi(xr[k]); }
    float sum = ((xf[0] + xf[1]) + (xf[2] + xf[3])) + ((xf[4] + xf[5]) + (xf[6] + xf[7]));
    const float mean = wave_sum(sum) * (1.f / DM);
    float sq = 0.f;
#pragma unroll
    for (int k = 0; k < 8; ++k) { const float d = xf[k] - mean; sq = fmaf(d, d, sq); }
    const float var = wave_sum(sq) * (1.f / DM);
    const float mb = rbf(mean), sd = rbf(__builtin_amdgcn_sqrtf(rbf(rbf(var) + 1e-5f)));
    const float rs = __builtin_amdgcn_rcpf(sd);
    f32x4 a, b;
#pragma unroll
    for (int k = 0; k < 4; ++k) { a[k] = rbf(rbf(xf[k] - mb) * rs); b[k] = rbf(rbf(xf[4 + k] - mb) * rs); }
    *(LAS f32x4*)(L + L_XN + 32 * lane) = a;
    *(LAS f32x4*)(L + L_XN + 32 * lane + 16) = b;
  }
  asm volatile("s_waitcnt lgkmcnt(0)" : "+v"(xo) :: "memory");
  float xq[4];
#pragma unroll
  for (int i = 0; i < 4; ++i) xq[i] = *(const LAS float*)(L + L_XN + 4 * (128 * (s >> 3) + trow + 8 * i));
  STAMP(1);
  for (unsigned spins = 0; LDSU(ctrl[1]) < 2u && spins < SPIN_CAP; ++spins) {}
  asm volatile("" ::: "memory");
  u32x4 wqr[4];
#pragma unroll
  for (int i = 0; i < 4; ++i) wqr[i] = *(const LAS u32x4*)(L + L_WO + (trow + 8 * i) * 128 + (lane & 7) * 16);

  LAS float* red = (LAS float*)(L + L_RED);
  {
    float acc[8];
#pragma unroll
    for (int k = 0; k < 8; ++k) acc[k] = 0.f;
#pragma unroll
    for (int i = 0; i < 4; ++i)
#pragma unroll
      for (int k = 0; k < 4; ++k) {
        acc[2 * k] = fmaf(xq[i], bflo(wqr[i][k]), acc[2 * k]);
        acc[2 * k + 1] = fmaf(xq[i], bfhi(wqr[i][k]), acc[2 * k + 1]);
      }
#pragma unroll
    for (int k = 0; k < 8; ++k) acc[k] = sum_mod8(acc[k]);
    if (lane < 8) {
      *(LAS f32x4*)(red + cw * 64 + 8 * lane) = f32x4{acc[0], acc[1], acc[2], acc[3]};
      *(LAS f32x4*)(red + cw * 64 + 8 * lane + 4) = f32x4{acc[4], acc[5], acc[6], acc[7]};
    }
  }
  asm volatile("s_waitcnt lgkmcnt(0)" ::: "memory");
  if (lane == 0) __hip_atomic_fetch_add((LAS unsigned*)(ctrl + 8), 1u, __ATOMIC_RELAXED, __HIP_MEMORY_SCOPE_WORKGROUP);
  if (cw == 0) {
    for (unsigned spins = 0; LDSU(ctrl[8]) < 4u && spins < SPIN_CAP; ++spins) {}
    asm volatile("" ::: "memory");
    if (lane == 0) ctrl[3] = 2u;
    if (lane < 32) {
      const f32x2 q2 = (*(const LAS f32x2*)(red + 2 * lane) + *(const LAS f32x2*)(red + 64 + 2 * lane)) +
                       (*(const LAS f32x2*)(red + 128 + 2 * lane) + *(const LAS f32x2*)(red + 192 + 2 * lane));
      st_gran(ws, W_Q + (unsigned)(((g * 8 + (s & 7)) * 4 + (s >> 3)) * 32 + lane) * 16, __float_as_uint(q2[0]), tlo, __float_as_uint(q2[1]), thi);
    }
  }
  STAMP(2);
  unsigned ka[4], va[8];
  {
    const unsigned rho = lane & 15, gg = lane >> 4;
    const unsigned ring0 = (unsigned)(size_t)(L + L_RING);
#pragma unroll
    for (int m = 0; m < 4; ++m) ka[m] = ring0 + rho * 256 + 16 * ((4 * m + gg) ^ rho);
    const unsigned q = (lane & 15) >> 2, p = lane & 3, row = 4 * gg + q;
#pragma unroll
    for (int b = 0; b < 8; ++b) va[b] = ring0 + 4096 + row * 256 + 8 * p + 32 * (b ^ (row & 7));
  }
  int slot = cw;
  auto await = [&](int k) -> bool {
    const unsigned j = 4 * k + cw;
    for (unsigned spins = 0; spins < SPIN_CAP; ++spins) {
      const u32x4 cv = *(volatile LAS u32x4*)(L + L_CTRL);
      if (LDSU(cv[0]) > j) return true;
      const unsigned tot = LDSU(cv[2]);
      if (tot != 0u && j >= tot) return false;
      __builtin_amdgcn_s_sleep(1);
    }
    return false;
  };
  auto fetch = [&](bf16x8 (&kf)[4], bf16x4 (&vf)[8]) {
    asm volatile("" ::: "memory");
    const unsigned sb = (unsigned)slot * SLOT;
    slot += 4; if (slot >= RING) slot -= RING;
#pragma unroll
    for (int m = 0; m < 4; ++m) kf[m] = *(const LAS bf16x8*)(size_t)(ka[m] + sb);
#pragma unroll
    for (int b = 0; b < 8; ++b) vf[b] = __builtin_amdgcn_ds_read_tr16_b64_v4bf16((LAS bf16x4*)(size_t)(va[b] + sb));
  };
  auto release = [&](int k) {
    asm volatile("s_waitcnt lgkmcnt(0)" ::: "memory");
    if (lane == 0) ctrl[4 + cw] = (unsigned)k + 1u;
  };
  bf16x8 kp[NPRE][4];
  bf16x4 vp[NPRE][8];
#pragma unroll
  for (int k = 0; k < NPRE; ++k) { (void)await(k); fetch(kp[k], vp[k]); release(k); }
#pragma unroll
  for (int k = 0; k < NPRE; ++k) {
#pragma unroll
    for (int m = 0; m < 4; ++m) asm volatile("" : "+v"(kp[k][m]));
#pragma unroll
    for (int b = 0; b < 8; ++b) asm volatile("" : "+v"(vp[k][b]));
  }

  {
    u32x4 qa[4] = {};
    const unsigned off = W_Q + (unsigned)((g * 8 + (t >> 5)) * 128 + (t & 31)) * 16;
    bool ok[4] = {false, false, false, false};
    for (unsigned spins = 0; spins < SPIN_CAP; ++spins) {
      asm volatile("" ::: "memory");
#pragma unroll
      for (int i = 0; i < 4; ++i) poll_gran(ws, off + i * 512, ok[i], qa[i]);
#pragma unroll
      for (int i = 0; i < 4; ++i) ok[i] = qa[i][1] == tlo && qa[i][3] == thi;
      if (__all(ok[0] && ok[1] && ok[2] && ok[3])) break;
      poll_pause(!(ok[0] && ok[1] && ok[2] && ok[3]));
    }
    const float q0 = (__uint_as_float(qa[0][0]) + __uint_as_float(qa[1][0])) + (__uint_as_float(qa[2][0]) + __uint_as_float(qa[3][0]));
    const float q1 = (__uint_as_float(qa[0][2]) + __uint_as_float(qa[1][2])) + (__uint_as_float(qa[2][2]) + __uint_as_float(qa[3][2]));
    *(LAS unsigned*)(L + L_Q + 4 * t) = pack2(q0, q1);
    asm volatile("s_waitcnt lgkmcnt(0)" ::: "memory");
    if (lane == 0) __hip_atomic_fetch_add((LAS unsigned*)(ctrl + 9), 1u, __ATOMIC_RELAXED, __HIP_MEMORY_SCOPE_WORKGROUP);
    for (unsigned spins = 0; LDSU(ctrl[9]) < 4u && spins < SPIN_CAP; ++spins) {}
  }
  asm volatile("" ::: "memory");
  STAMP(3);
  bf16x8 qf[4];
  {
    const int col = lane & 15, gg = lane >> 4;
#pragma unroll
    for (int m = 0; m < 4; ++m) qf[m] = *(const LAS bf16x8*)(L + L_Q + (col & 3) * 256 + 64 * m + 16 * gg);
  }

  constexpr float CS = 0.08838834764831845f * 1.4426950408889634f;
  f32x4 acc[8], accl = {0.f, 0.f, 0.f, 0.f};
#pragma unroll
  for (int b = 0; b < 8; ++b) acc[b] = f32x4{0.f, 0.f, 0.f, 0.f};
  float mrun = -INFINITY, mc = 0.f;
  const s16x4 ones = {0x3F80, 0x3F80, 0x3F80, 0x3F80};
  auto scores = [&](const bf16x8 (&kf)[4]) -> s16x4 {
    f32x4 sc = {0.f, 0.f, 0.f, 0.f};
#pragma unroll
    for (int m = 0; m < 4; ++m) sc = __builtin_amdgcn_mfma_f32_16x16x32_bf16(kf[m], qf[m], sc, 0, 0, 0);
    const float mx = fmaxf(fmaxf(sc[0], sc[1]), fmaxf(sc[2], sc[3]));
    if (__any(mx > mrun)) {
      const float mn = fmaxf(mrun, xrow_max(mx));
      const float alpha = __builtin_amdgcn_exp2f((mrun - mn) * CS);
      mrun = mn; mc = mn * CS;
#pragma unroll
      for (int b = 0; b < 8; ++b) acc[b] *= alpha;
      accl *= alpha;
    }
    const float p0 = __builtin_amdgcn_exp2f(fmaf(sc[0], CS, -mc)), p1 = __builtin_amdgcn_exp2f(fmaf(sc[1], CS, -mc));
    const float p2 = __builtin_amdgcn_exp2f(fmaf(sc[2], CS, -mc)), p3 = __builtin_amdgcn_exp2f(fmaf(sc[3], CS, -mc));
    const u32x2 pp = {pack2(p0, p1), pack2(p2, p3)};
    return __builtin_bit_cast(s16x4, pp);
  };
  auto pv = [&](const bf16x4 (&vf)[8], s16x4 pf) {
    accl = __builtin_amdgcn_mfma_f32_16x16x16bf16_1k(ones, pf, accl, 0, 0, 0);
#pragma unroll
    for (int b = 0; b < 8; ++b) acc[b] = __builtin_amdgcn_mfma_f32_16x16x16bf16_1k(__builtin_bit_cast(s16x4, vf[b]), pf, acc[b], 0, 0, 0);
  };
  STAMP(4);
#pragma unroll
  for (int k = 0; k < NPRE; ++k) pv(vp[k], scores(kp[k]));
  const int kw = ((nstat - cw + 3) >> 2) - 1;
  for (int k = NPRE; k < kw; ++k) {
    bf16x8 kf[4];
    bf16x4 vf[8];
    (void)await(k);
    fetch(kf, vf);
    const s16x4 pf = scores(kf);
    release(k);
    pv(vf, pf);
  }
  for (unsigned spins = 0; LDSU(ctrl[1]) < (has_job ? 20u : 4u) && spins < SPIN_CAP; ++spins) __builtin_amdgcn_s_sleep(1);
  asm volatile("" ::: "memory");
  f32x2 wof[16];
#pragma unroll
  for (int r = 0; r < 16; ++r) {
    const unsigned u = *(const LAS unsigned*)(L + L_WO + r * 1024 + 4 * t);
    wof[r] = f32x2{bflo(u), bfhi(u)};
  }
#pragma unroll
  for (int r = 0; r < 16; ++r) asm volatile("" : "+v"(wof[r]));
  unsigned o1off = W_O + (unsigned)((((g * 4 + cw) * 8 + (lane >> 3)) * 32 + s) * 8 + (lane & 7)) * 16;
  unsigned ooff = W_O + (unsigned)(((g * 4 + (s >> 3)) * 8 + (s & 7)) * 256 + t) * 16;
  unsigned soff_out = W_S + (unsigned)(((t / GPF) * 256 + c) * GPF + t % GPF) * 16;
  unsigned mgw = (unsigned)(size_t)(L + L_MRG) + 4 * ((cw * 4 + (lane & 3)) * 128 + 32 * ((lane >> 2) & 3) + 4 * (lane >> 4));
  unsigned mgr = (unsigned)(size_t)(L + L_MRG) + 4 * (cw * 128 + 2 * lane);
  asm volatile("" : "+v"(o1off), "+v"(ooff), "+v"(soff_out), "+v"(mgw), "+v"(mgr));
  for (int k = kw; await(k); ++k) {
    bf16x8 kf[4];
    bf16x4 vf[8];
    fetch(kf, vf);
    const s16x4 pf = scores(kf);
    release(k);
    pv(vf, pf);
  }
  STAMP(5);

  {
    const int rep = (lane >> 2) & 3;
    const f32x4 va0 = rep == 0 ? acc[0] : rep == 1 ? acc[2] : rep == 2 ? acc[4] : acc[6];
    const f32x4 va1 = rep == 0 ? acc[1] : rep == 1 ? acc[3] : rep == 2 ? acc[5] : acc[7];
    *(LAS f32x4*)(size_t)(mgw) = va0;
    *(LAS f32x4*)(size_t)(mgw + 64) = va1;
    if (lane < 4) *(LAS f32x2*)(L + L_ML + (lane * 4 + cw) * 8) = f32x2{mc, accl[0]};
  }
  asm volatile("s_waitcnt lgkmcnt(0)" ::: "memory");
  if (lane == 0) __hip_atomic_fetch_add((LAS unsigned*)(ctrl + 12), 1u, __ATOMIC_RELAXED, __HIP_MEMORY_SCOPE_WORKGROUP);
  for (unsigned spins = 0; LDSU(ctrl[12]) < 4u && spins < SPIN_CAP; ++spins) {}
  asm volatile("" ::: "memory");
  {
    const unsigned tb = thi & 0xffff0000u;
    const int head = cw;
    const f32x4 m01 = *(const LAS f32x4*)(L + L_ML + head * 32), m23 = *(const LAS f32x4*)(L + L_ML + head * 32 + 16);
    const f32x2 o0 = *(const LAS f32x2*)(size_t)(mgr), o1 = *(const LAS f32x2*)(size_t)(mgr + 2048);
    const f32x2 o2 = *(const LAS f32x2*)(size_t)(mgr + 4096), o3 = *(const LAS f32x2*)(size_t)(mgr + 6144);
    const float M = fmaxf(fmaxf(m01[0], m01[2]), fmaxf(m23[0], m23[2]));
    const float f0 = __builtin_amdgcn_exp2f(m01[0] - M), f1 = __builtin_amdgcn_exp2f(m01[2] - M);
    const float f2 = __builtin_amdgcn_exp2f(m23[0] - M), f3 = __builtin_amdgcn_exp2f(m23[2] - M);
    const float Ls = fmaf(f0, m01[1], f1 * m01[3]) + fmaf(f2, m23[1], f3 * m23[3]);
    const float rl = __builtin_amdgcn_rcpf(Ls);
    const f32x2 o = (f0 * o0 + f1 * o1 + (f2 * o2 + f3 * o3)) * rl;
    const unsigned lam = __float_as_uint(M + __builtin_amdgcn_logf(Ls));
    st_gran(ws, o1off, __float_as_uint(o[0]), tlo, __float_as_uint(o[1]), tb | ((lane & 1) ? (lam & 0xffffu) : (lam >> 16)));
  }
  STAMP(6);

  u32x4 sg[GPF] = {};
  const unsigned soff = W_S + (unsigned)((fin * 256 + t / GPF) * GPF + t % GPF) * 16;

  {
    const unsigned tb = thi & 0xffff0000u;
    u32x4 og = {};
    bool ok = false;
    if (DELAY1) __builtin_amdgcn_s_sleep(DELAY1);
    for (unsigned spins = 0; spins < SPIN_CAP; ++spins) {
      asm volatile("" ::: "memory");
      poll_gran(ws, ooff, ok, og);
      ok = og[1] == tlo && (og[3] & 0xffff0000u) == tb;
      if (__all(ok)) break;
      poll_pause(!ok);
    }
    STAMP(7);
    const unsigned mine = og[3] & 0xffffu, other = dppu<XOR1>(mine);
    const float lam = __uint_as_float((lane & 1) ? ((other << 16) | mine) : ((mine << 16) | other));
    const float Mw = max_mod8(lam);
    const float wt = __builtin_amdgcn_exp2f(lam - Mw);
    const float a0 = sum_mod8(wt * __uint_as_float(og[0])), a1 = sum_mod8(wt * __uint_as_float(og[2])), Ww = sum_mod8(wt);
    LAS float* r2 = (LAS float*)(L + L_RED);
    if (lane < 8) { r2[(2 * lane) * 4 + cw] = a0; r2[(2 * lane + 1) * 4 + cw] = a1; }
    if (lane == 8) { r2[64 + cw] = Mw; r2[68 + cw] = Ww; }
    asm volatile("s_waitcnt lgkmcnt(0)" ::: "memory");
    __builtin_amdgcn_s_barrier();
    const f32x4 ov = *(const LAS f32x4*)(r2 + 4 * (lane & 15)), mv = *(const LAS f32x4*)(r2 + 64), wv = *(const LAS f32x4*)(r2 + 68);
    const float M = fmaxf(fmaxf(mv[0], mv[1]), fmaxf(mv[2], mv[3]));
    const float f0 = __builtin_amdgcn_exp2f(mv[0] - M), f1 = __builtin_amdgcn_exp2f(mv[1] - M);
    const float f2 = __builtin_amdgcn_exp2f(mv[2] - M), f3 = __builtin_amdgcn_exp2f(mv[3] - M);
    const float num = fmaf(f0, ov[0], f1 * ov[1]) + fmaf(f2, ov[2], f3 * ov[3]);
    const float den = fmaf(f0, wv[0], f1 * wv[1]) + fmaf(f2, wv[2], f3 * wv[3]);
    const float od = rbf(num * __builtin_amdgcn_rcpf(den));
    f32x2 sa = {0.f, 0.f}, sb = {0.f, 0.f};
#pragma unroll
    for (int r = 0; r < 16; r += 2) {
      const float oa = __builtin_bit_cast(float, __builtin_amdgcn_readlane(__builtin_bit_cast(int, od), r));
      const float ob = __builtin_bit_cast(float, __builtin_amdgcn_readlane(__builtin_bit_cast(int, od), r + 1));
      sa += oa * wof[r]; sb += ob * wof[r + 1];
    }
    const f32x2 sv = sa + sb;
    st_gran(ws, soff_out, __float_as_uint(sv[0]), tlo, __float_as_uint(sv[1]), thi);
  }
  STAMP(8);

  if (is_fin) {
    bool sok[GPF];
#pragma unroll
    for (int i = 0; i < GPF; ++i) sok[i] = EARLY_S && sg[i][1] == tlo && sg[i][3] == thi;
    if (DELAY2) __builtin_amdgcn_s_sleep(DELAY2);
    for (unsigned spins = 0; spins < SPIN_CAP; ++spins) {
      bool all = true;
#pragma unroll
      for (int i = 0; i < GPF; ++i) all = all && sok[i];
      if (__all(all)) break;
      if (spins || EARLY_S) poll_pause(!all);
      asm volatile("" ::: "memory");
#pragma unroll
      for (int i = 0; i < GPF; ++i) poll_gran(ws, soff + i * 4096, sok[i], sg[i]);
#pragma unroll
      for (int i = 0; i < GPF; ++i) sok[i] = sg[i][1] == tlo && sg[i][3] == thi;
    }
    STAMP(9);
    float a0 = 0.f, a1 = 0.f;
#pragma unroll
    for (int i = 0; i < GPF; i += 2) {
      a0 += __uint_as_float(sg[i][0]) + __uint_as_float(sg[i + 1][0]);
      a1 += __uint_as_float(sg[i][2]) + __uint_as_float(sg[i + 1][2]);
    }
    a0 = GPF == 4 ? sum_mod4(a0) : sum_mod8(a0); a1 = GPF == 4 ? sum_mod4(a1) : sum_mod8(a1);
    LAS float* r3 = (LAS float*)(L + L_RED) + 96;
    if (lane < GPF) { r3[(2 * lane) * 4 + cw] = a0; r3[(2 * lane + 1) * 4 + cw] = a1; }
    asm volatile("s_waitcnt lgkmcnt(0)" ::: "memory");
    __builtin_amdgcn_s_barrier();
    if (t < GPF) {
      const f32x4 u0 = *(const LAS f32x4*)(r3 + 8 * t), u1 = *(const LAS f32x4*)(r3 + 8 * t + 4);
      const f32x2 gv = *(const LAS f32x2*)(L + L_RED + 768 + 8 * t);
      const float s0 = rbf((u0[0] + u0[1]) + (u0[2] + u0[3])), s1 = rbf((u1[0] + u1[1]) + (u1[2] + u1[3]));
      unsigned res = pack2(bflo(xo) + rbf(s0 * gv[0]), bfhi(xo) + rbf(s1 * gv[1]));
      *(GAS unsigned*)(out + 2 * GPF * fin + 2 * t) = res;
      if (fin == 0 && t == 0) __builtin_amdgcn_raw_buffer_store_b64(u32x2{tlo, thi}, ws, W_EPOCH, 0, 0);
    }
    STAMP(10);
  }
  STAMP_FLUSH();
}

}

extern "C" void kernel_launch(void* const* d_in, const int* in_sizes, int n_in, void* d_out, int out_size, void* d_ws, size_t ws_size,
                              hipStream_t stream) {
  (void)in_sizes; (void)n_in; (void)out_size; (void)ws_size;
  (void)hipFuncSetAttribute((const void*)fused_decode_kernel, hipFuncAttributeMaxDynamicSharedMemorySize, L_END);
  hipLaunchKernelGGL(fused_decode_kernel, dim3(NWG), dim3(NTHR), L_END, stream,
                     (const unsigned short*)d_in[0], (const unsigned short*)d_in[1], (const unsigned short*)d_in[2],
                     (const unsigned short*)d_in[3], (const unsigned short*)d_in[4], (const unsigned short*)d_in[6],
                     (const unsigned short*)d_in[7], (unsigned short*)d_out, (unsigned char*)d_ws);
}
```

```cpp
#include <hip/hip_runtime.h>
#include <hip/hip_bf16.h>
#include <stdint.h>

#define LAS __attribute__((address_space(3)))
#define GAS __attribute__((address_space(1)))
typedef unsigned u32x4 __attribute__((ext_vector_type(4)));
typedef unsigned u32x2 __attribute__((ext_vector_type(2)));
typedef int i32x4 __attribute__((ext_vector_type(4)));
typedef float f32x4 __attribute__((ext_vector_type(4)));
typedef float f32x2 __attribute__((ext_vector_type(2)));
typedef __bf16 bf16x8 __attribute__((ext_vector_type(8)));
typedef __bf16 bf16x4 __attribute__((ext_vector_type(4)));
typedef __bf16 bf16x2 __attribute__((ext_vector_type(2)));
typedef short s16x4 __attribute__((ext_vector_type(4)));

namespace {

constexpr int DM = 512, HKV = 8, SK = 65536, DH = 128, DFF = 1792;
constexpr int NWG = 256, NTHR = 384;
constexpr unsigned KV_BYTES = (unsigned)HKV * SK * DH * 2;

constexpr int RING = 16, SLOT = 8192;
constexpr int WAITN = 16;
constexpr int INFL = WAITN / 8 + 1;
constexpr int NBURST = 8;
constexpr int NPRE = 3;
constexpr int NJOB = DFF / 64;
constexpr int FFN_SHED = 21;
constexpr int JOB_FIRST = 12, JOB_STEP = 6, WO_FIRST = 24, WO_STEP = 6;
constexpr int GPF = 4;
constexpr int FPG = 256 / GPF / 8;
constexpr int DELAY1 = 28, DELAY2 = 28;

constexpr int L_RING = 0;
constexpr int L_WO = RING * SLOT;
constexpr int L_CTRL = L_WO + 16384;
constexpr int L_XN = L_CTRL + 256;
constexpr int L_Q = L_XN + 2048;
constexpr int L_RED = L_Q + 1024;
constexpr int L_FT = L_RED + 1024;
constexpr int L_MRG = L_FT + 2048;
constexpr int L_ML = L_MRG + 8192;
constexpr int L_END = L_ML + 256;
static_assert(L_END <= 160 * 1024, "LDS");

constexpr unsigned W_EPOCH = 0;
constexpr unsigned W_Q = 4096;
constexpr unsigned W_O = W_Q + 8 * 8 * 4 * 32 * 16;
constexpr unsigned W_S = W_O + 8 * 4 * 8 * 32 * 8 * 16;
constexpr unsigned W_F = W_S + 256 * 256 * 16;
constexpr unsigned W_END = W_F + NJOB * 256 * 16;

constexpr unsigned SPIN_CAP = 1u << 20;
#define LDSU(x) ((unsigned)__builtin_amdgcn_readfirstlane(x))

__device__ __forceinline__ float bf2f(unsigned short b) { return __builtin_bit_cast(float, (unsigned)b << 16); }
__device__ __forceinline__ float bflo(unsigned u) { return __builtin_bit_cast(float, u << 16); }
__device__ __forceinline__ float bfhi(unsigned u) { return __builtin_bit_cast(float, u & 0xffff0000u); }
__device__ __forceinline__ unsigned short f2bf(float f) { return __builtin_bit_cast(unsigned short, (__bf16)f); }
__device__ __forceinline__ float rbf(float f) { return bf2f(f2bf(f)); }
__device__ __forceinline__ unsigned pack2(float lo, float hi) {
  const f32x2 v = {lo, hi};
  return __builtin_bit_cast(unsigned, __builtin_convertvector(v, bf16x2));
}

template <int CTRL> __device__ __forceinline__ float dpp(float x) {
  return __builtin_bit_cast(float, __builtin_amdgcn_mov_dpp(__builtin_bit_cast(int, x), CTRL, 0xf, 0xf, true));
}
template <int CTRL> __device__ __forceinline__ unsigned dppu(unsigned x) {
  return (unsigned)__builtin_amdgcn_mov_dpp((int)x, CTRL, 0xf, 0xf, true);
}
constexpr int XOR1 = 0xB1, XOR2 = 0x4E, ROR4 = 0x124, ROR8 = 0x128;
__device__ __forceinline__ float xrow_sum(float x) {
  auto s = __builtin_amdgcn_permlane16_swap(__float_as_uint(x), __float_as_uint(x), false, false);
  x = __uint_as_float(s[0]) + __uint_as_float(s[1]);
  auto t = __builtin_amdgcn_permlane32_swap(__float_as_uint(x), __float_as_uint(x), false, false);
  return __uint_as_float(t[0]) + __uint_as_float(t[1]);
}
__device__ __forceinline__ float xrow_max(float x) {
  auto s = __builtin_amdgcn_permlane16_swap(__float_as_uint(x), __float_as_uint(x), false, false);
  x = fmaxf(__uint_as_float(s[0]), __uint_as_float(s[1]));
  auto t = __builtin_amdgcn_permlane32_swap(__float_as_uint(x), __float_as_uint(x), false, false);
  return fmaxf(__uint_as_float(t[0]), __uint_as_float(t[1]));
}
__device__ __forceinline__ float wave_sum(float x) {
  x += dpp<XOR1>(x); x += dpp<XOR2>(x); x += dpp<ROR4>(x); x += dpp<ROR8>(x);
  return xrow_sum(x);
}
__device__ __forceinline__ float sum_mod4(float x) {
  x += dpp<ROR4>(x); x += dpp<ROR8>(x);
  return xrow_sum(x);
}
__device__ __forceinline__ float sum_mod8(float x) { x += dpp<ROR8>(x); return xrow_sum(x); }
__device__ __forceinline__ float max_mod8(float x) { x = fmaxf(x, dpp<ROR8>(x)); return xrow_max(x); }

__device__ __forceinline__ void lds_count(volatile LAS unsigned* p, int lane) {
  if (lane == 0) asm volatile("ds_add_u32 %0, %1" :: "v"((unsigned)(size_t)p), "v"(1u) : "memory");
}

__device__ __forceinline__ void poll_pause(bool missing) {
  const int n = __builtin_popcountll(__ballot(missing));
  if (n > 32) __builtin_amdgcn_s_sleep(4);
  else if (n > 12) __builtin_amdgcn_s_sleep(2);
}

__device__ __forceinline__ void st_gran(__amdgpu_buffer_rsrc_t ws, unsigned off, unsigned a, unsigned b, unsigned c, unsigned d) {
  const u32x4 g = {a, b, c, d};
  __builtin_amdgcn_raw_buffer_store_b128(g, ws, off, 0, 16);
}
__device__ __forceinline__ u32x4 ld_gran(__amdgpu_buffer_rsrc_t ws, unsigned off) {
  return __builtin_amdgcn_raw_buffer_load_b128(ws, off, 0, 16);
}
__device__ __forceinline__ void poll_gran(__amdgpu_buffer_rsrc_t ws, unsigned off, bool have, u32x4& g) {
  const u32x4 v = __builtin_amdgcn_raw_buffer_load_b128(ws, have ? 0x7ffffff0u : off, 0, 16);
#pragma unroll
  for (int k = 0; k < 4; ++k) g[k] = have ? g[k] : v[k];
}

__device__ __forceinline__ int job_of(int g, int s) { return g * 4 + (s >> 3); }

__global__ void __launch_bounds__(NTHR, 2)
fused_decode_kernel(const unsigned short* __restrict__ x, const unsigned short* __restrict__ kc, const unsigned short* __restrict__ vc,
                    const unsigned short* __restrict__ wq, const unsigned short* __restrict__ wo, const unsigned short* __restrict__ w1,
                    const unsigned short* __restrict__ w2, unsigned short* __restrict__ out, unsigned char* __restrict__ wsp) {
  extern __shared__ __attribute__((aligned(16))) unsigned char lds[];
  LAS unsigned char* L = (LAS unsigned char*)lds;
  volatile LAS unsigned* ctrl = (volatile LAS unsigned*)(L + L_CTRL);
  const int w = blockIdx.x, g = w & 7, s = w >> 3;
  const int wave = __builtin_amdgcn_readfirstlane(threadIdx.x >> 6), lane = threadIdx.x & 63;
  constexpr int NF_J = 128 - FFN_SHED, NF_O = 128 + FFN_SHED / 7;
  const bool has_job = g < 7 && (s & 7) == 2;
  const int fill0 = g == 7 ? 128 * s : NF_O * s - (NF_O - NF_J) * ((s + 5) >> 3);
  const int nfill = g == 7 ? 128 : has_job ? NF_J : NF_O;

  if (wave == 0) {
    const unsigned long long kb = (unsigned long long)kc, vb = (unsigned long long)vc;
    const i32x4 sk = {(int)kb, (int)(kb >> 32), (int)KV_BYTES, 0x00020000};
    const i32x4 sv = {(int)vb, (int)(vb >> 32), (int)KV_BYTES, 0x00020000};
    const unsigned base = (unsigned)g * (SK * DH * 2) + (unsigned)fill0 * 4096;
    const unsigned r4 = lane >> 4, p = lane & 15;
    const unsigned k0 = r4 * 256 + 16 * (p ^ (0 + r4)), k1 = r4 * 256 + 16 * (p ^ (4 + r4));
    const unsigned k2 = r4 * 256 + 16 * (p ^ (8 + r4)), k3 = r4 * 256 + 16 * (p ^ (12 + r4));
    const unsigned v0 = r4 * 256 + 16 * (p ^ (r4 << 1)), v1 = r4 * 256 + 16 * (p ^ ((4 + r4) << 1));
    const unsigned ring0 = (unsigned)(size_t)(L + L_RING);
    __builtin_amdgcn_s_setprio(3);
    const int naux = has_job ? 20 : 4, job = job_of(g, s);
    const unsigned vq = (lane >> 3) * 8192 + (lane & 7) * 16, v1w = (lane >> 3) * (DFF * 2) + (lane & 7) * 16, vc16 = lane * 16;
    const unsigned aux0 = (unsigned)(size_t)(L + L_WO);
    const unsigned long long qb = (unsigned long long)wq, ob = (unsigned long long)wo, b1 = (unsigned long long)w1, b2 = (unsigned long long)w2;
    const i32x4 sq = {(int)qb, (int)(qb >> 32), DM * 4096 * 2, 0x00020000}, so = {(int)ob, (int)(ob >> 32), 4096 * DM * 2, 0x00020000};
    const i32x4 s1 = {(int)b1, (int)(b1 >> 32), DM * DFF * 2, 0x00020000}, s2 = {(int)b2, (int)(b2 >> 32), DM * DFF * 2, 0x00020000};
    if (lane < 16) ctrl[lane] = 0u;
#define KV_ISSUE(WAIT) asm volatile( \
            WAIT "s_mov_b32 m0, %0\n\ts_nop 0\n\t" \
            "buffer_load_dwordx4 %2, %8, %10 offen nt lds\n\t" \
            "buffer_load_dwordx4 %3, %8, %10 offen offset:1024 nt lds\n\t" \
            "buffer_load_dwordx4 %4, %8, %10 offen offset:2048 nt lds\n\t" \
            "buffer_load_dwordx4 %5, %8, %10 offen offset:3072 nt lds\n\t" \
            "s_mov_b32 m0, %1\n\ts_nop 0\n\t" \
            "buffer_load_dwordx4 %6, %9, %10 offen nt lds\n\t" \
            "buffer_load_dwordx4 %7, %9, %10 offen offset:1024 nt lds\n\t" \
            "buffer_load_dwordx4 %6, %9, %10 offen offset:2048 nt lds\n\t" \
            "buffer_load_dwordx4 %7, %9, %10 offen offset:3072 nt lds" \
            :: "s"(m0k), "s"(m0k + 4096), "v"(k0), "v"(k1), "v"(k2), "v"(k3), "v"(v0), "v"(v1), "s"(sk), "s"(sv), "s"(sko), \
               "i"(WAITN) : "memory")
#define SIDE_ISSUE(SRD, VO, SO0, RSTEP) do { \
          const unsigned so0_ = (SO0), st_ = (RSTEP) - 1024, so4_ = so0_ + 4 * (RSTEP); \
          asm volatile( \
            "s_waitcnt vmcnt(%12)\n\t" \
            "s_mov_b32 m0, %0\n\ts_nop 0\n\t" \
            "buffer_load_dwordx4 %2, %3, %4 offen nt lds\n\t" \
            "buffer_load_dwordx4 %2, %3, %5 offen offset:1024 nt lds\n\t" \
            "buffer_load_dwordx4 %2, %3, %6 offen offset:2048 nt lds\n\t" \
            "buffer_load_dwordx4 %2, %3, %7 offen offset:3072 nt lds\n\t" \
            "s_mov_b32 m0, %1\n\ts_nop 0\n\t" \
            "buffer_load_dwordx4 %2, %3, %8 offen nt lds\n\t" \
            "buffer_load_dwordx4 %2, %3, %9 offen offset:1024 nt lds\n\t" \
            "buffer_load_dwordx4 %2, %3, %10 offen offset:2048 nt lds\n\t" \
            "buffer_load_dwordx4 %2, %3, %11 offen offset:3072 nt lds" \
            :: "s"(dst), "s"(dst + 4096), "v"(VO), "s"(SRD), "s"(so0_), "s"(so0_ + st_), "s"(so0_ + 2 * st_), "s"(so0_ + 3 * st_), \
               "s"(so4_), "s"(so4_ + st_), "s"(so4_ + 2 * st_), "s"(so4_ + 3 * st_), "i"(WAITN) : "memory"); } while (0)
    auto side_pos = [&](int a) -> int { return a >= naux ? 0x7fffffff : a < 2 ? a : has_job ? JOB_FIRST + JOB_STEP * (a - 2) : WO_FIRST + WO_STEP * (a - 2); };
    unsigned m0k = ring0, sko = base;
#pragma unroll
    for (int a = 0; a < 2; ++a) {
      const unsigned dst = aux0 + (unsigned)a * 8192;
      SIDE_ISSUE(sq, vq, (unsigned)(128 * (s >> 3) + 64 * a) * 8192 + g * 1024 + (s & 7) * 128, 8 * 8192u);
    }
    asm volatile("s_waitcnt lgkmcnt(0)" ::: "memory");
    __builtin_amdgcn_s_barrier();
#pragma unroll
    for (int j = 0; j < NBURST - 2; ++j) { KV_ISSUE(""); m0k += SLOT; sko += 4096; }
    asm volatile("s_waitcnt vmcnt(%0)" :: "i"(8 * (NBURST - 2)) : "memory");
    *(volatile LAS u32x2*)(L + L_CTRL) = u32x2{0u, 2u};
    int kv = NBURST - 2, ax = 2, e = NBURST, le = 2, axl = 2, lim = RING, alim = 2;
    int nextpos = side_pos(2), nextland = nextpos;
    const int nent = nfill + naux;
    const unsigned ring_end = ring0 + RING * SLOT;
    u32x4 dn = {0u, 0u, 0u, 0u};
    unsigned da = 0u;
#define ACCOUNT() do { \
        le = max(le, e + 1 - INFL); \
        if (le > nextland) { ++axl; nextland = side_pos(axl); } \
        *(volatile LAS u32x2*)(L + L_CTRL) = u32x2{(unsigned)(le - axl), (unsigned)axl}; \
        lim = max(lim, RING + 4 * (int)LDSU(min(min(dn[0], dn[1]), min(dn[2], dn[3])))); \
        alim = max(alim, 2 + (int)LDSU(da)); \
        dn = *(volatile LAS u32x4*)(L + L_CTRL + 16); \
        da = ctrl[3]; \
      } while (0)
#define PARK(COND) do { \
        asm volatile("s_waitcnt vmcnt(0)" ::: "memory"); \
        le = e; while (le > nextland) { ++axl; nextland = side_pos(axl); } \
        *(volatile LAS u32x2*)(L + L_CTRL) = u32x2{(unsigned)(le - axl), (unsigned)axl}; \
        for (unsigned spins = 0; spins < SPIN_CAP; ++spins) { if (COND) break; __builtin_amdgcn_s_sleep(1); } \
        asm volatile("" ::: "memory"); \
      } while (0)
    while (e < nent) {
      const int stop = min(nextpos, nent);
      for (; e < stop; ++e) {
        if (kv >= lim) { PARK(LDSU(ctrl[4 + ((kv - RING) & 3)]) > (unsigned)((kv - RING) >> 2)); lim = kv + 1; }
        KV_ISSUE("s_waitcnt vmcnt(%11)\n\t");
        m0k += SLOT; if (m0k == ring_end) m0k = ring0;
        sko += 4096;
        ++kv;
        ACCOUNT();
      }
      if (e < nent) {
        if (ax >= alim) { PARK(LDSU(ctrl[3]) + 2u > (unsigned)ax); alim = ax + 1; }
        const unsigned dst = aux0 + (unsigned)(ax & 1) * 8192;
        if (ax >= naux - 2) SIDE_ISSUE(so, vc16, (unsigned)(16 * (g * 32 + s) + 8 * (ax - (naux - 2))) * 1024, 1024u);
        else if (ax < 10) SIDE_ISSUE(s1, v1w, (unsigned)(64 * (ax - 2)) * (DFF * 2) + job * 128, 8u * DFF * 2);
        else SIDE_ISSUE(s2, vc16, (unsigned)(64 * job + 8 * (ax - 10)) * 1024, 1024u);
        ++ax;
        nextpos = side_pos(ax);
        ACCOUNT();
        ++e;
      }
    }
#pragma unroll
    for (int r = INFL - 1; r >= 0; --r) {
      asm volatile("s_waitcnt vmcnt(%0)" :: "i"(8 * r) : "memory");
      le = max(le, e - r);
      while (le > nextland) { ++axl; nextland = side_pos(axl); }
      *(volatile LAS u32x2*)(L + L_CTRL) = u32x2{(unsigned)(le - axl), (unsigned)axl};
    }
#undef KV_ISSUE
#undef SIDE_ISSUE
#undef ACCOUNT
#undef PARK
    return;
  }

  const __amdgpu_buffer_rsrc_t ws = __builtin_amdgcn_make_buffer_rsrc((void*)wsp, 0, (int)W_END, 0x00020000);
  const int c = g * 32 + s;
  const bool is_fin = s % (32 / FPG) == 1;
  const int fin = g * FPG + s / (32 / FPG);

  if (wave == 5) {
    const u32x2 ep = __builtin_amdgcn_raw_buffer_load_b64(ws, W_EPOCH, 0, 16);
    __builtin_amdgcn_s_barrier();
    for (unsigned spins = 0; LDSU(ctrl[8]) < 4u && spins < SPIN_CAP; ++spins) __builtin_amdgcn_s_sleep(4);
    asm volatile("" ::: "memory");
    const unsigned long long tag = (((unsigned long long)ep[1] << 32) | ep[0]) + 1ull;
    const unsigned tlo = __builtin_amdgcn_readfirstlane((unsigned)tag), thi = __builtin_amdgcn_readfirstlane((unsigned)(tag >> 32));
    if (has_job) {
      const int job = job_of(g, s);
      const int r8 = lane >> 3, cq = lane & 7;
      float h[8];
#pragma unroll
      for (int k = 0; k < 8; ++k) h[k] = 0.f;
#pragma unroll 1
      for (int p = 0; p < 8; ++p) {
        for (unsigned spins = 0; LDSU(ctrl[1]) < (unsigned)(3 + p) && spins < SPIN_CAP; ++spins) __builtin_amdgcn_s_sleep(4);
        asm volatile("" ::: "memory");
        const LAS unsigned char* src = L + L_WO + (p & 1) * 8192 + r8 * 128 + cq * 16;
#pragma unroll
        for (int i = 0; i < 8; ++i) {
          const u32x4 wr = *(const LAS u32x4*)(src + i * 1024);
          const float xn = *(const LAS float*)(L + L_XN + 4 * (64 * p + 8 * i + r8));
#pragma unroll
          for (int k = 0; k < 4; ++k) { h[2 * k] = fmaf(xn, bflo(wr[k]), h[2 * k]); h[2 * k + 1] = fmaf(xn, bfhi(wr[k]), h[2 * k + 1]); }
        }
        asm volatile("s_waitcnt lgkmcnt(0)" ::: "memory");
        if (lane == 0) ctrl[3] = (unsigned)(3 + p);
      }
#pragma unroll
      for (int k = 0; k < 8; ++k) h[k] = fmaxf(rbf(sum_mod8(h[k])), 0.f);
      float bp[8];
#pragma unroll
      for (int k = 0; k < 8; ++k) bp[k] = 0.f;
#pragma unroll 1
      for (int p = 0; p < 8; ++p) {
        for (unsigned spins = 0; LDSU(ctrl[1]) < (unsigned)(11 + p) && spins < SPIN_CAP; ++spins) __builtin_amdgcn_s_sleep(4);
        asm volatile("" ::: "memory");
        const LAS unsigned char* src = L + L_WO + (p & 1) * 8192 + lane * 16;
#pragma unroll
        for (int i = 0; i < 8; ++i) {
          const u32x4 wr = *(const LAS u32x4*)(src + i * 1024);
          const float hv = __builtin_bit_cast(float, __builtin_amdgcn_readlane(__builtin_bit_cast(int, h[i]), p));
#pragma unroll
          for (int k = 0; k < 4; ++k) { bp[2 * k] = fmaf(hv, bflo(wr[k]), bp[2 * k]); bp[2 * k + 1] = fmaf(hv, bfhi(wr[k]), bp[2 * k + 1]); }
        }
        asm volatile("s_waitcnt lgkmcnt(0)" ::: "memory");
        if (lane == 0) ctrl[3] = (unsigned)(11 + p);
      }
      *(LAS f32x4*)(L + L_FT + 32 * lane) = f32x4{bp[0], bp[1], bp[2], bp[3]};
      *(LAS f32x4*)(L + L_FT + 32 * lane + 16) = f32x4{bp[4], bp[5], bp[6], bp[7]};
      asm volatile("s_waitcnt lgkmcnt(0)" ::: "memory");
#pragma unroll
      for (int e = 0; e < 4; ++e) {
        const f32x2 v = *(const LAS f32x2*)(L + L_FT + 8 * (64 * e + lane));
        st_gran(ws, W_F + (unsigned)(job * 256 + 64 * e + lane) * 16, __float_as_uint(v[0]), tlo, __float_as_uint(v[1]), thi);
      }
    }
    if (is_fin) {
      for (unsigned spins = 0; (int)LDSU(ctrl[0]) < nfill && spins < SPIN_CAP; ++spins) __builtin_amdgcn_s_sleep(8);
      constexpr int JPL = 64 / GPF, NL = (NJOB + JPL - 1) / JPL;
      u32x4 fg[NL];
      const unsigned oa = W_F + (unsigned)((lane / GPF) * 256 + fin * GPF + (lane % GPF)) * 16;
      for (unsigned spins = 0; spins < SPIN_CAP; ++spins) {
        asm volatile("" ::: "memory");
        bool ok = true;
#pragma unroll
        for (int i = 0; i < NL; ++i) fg[i] = ld_gran(ws, lane / GPF + JPL * i < NJOB ? oa + i * JPL * 256 * 16 : oa);
#pragma unroll
        for (int i = 0; i < NL; ++i) ok = ok && fg[i][1] == tlo && fg[i][3] == thi;
        if (__all(ok)) break;
        __builtin_amdgcn_s_sleep(8);
      }
      float b0 = 0.f, b1 = 0.f;
#pragma unroll
      for (int i = 0; i < NL; ++i) if (lane / GPF + JPL * i < NJOB) { b0 += __uint_as_float(fg[i][0]); b1 += __uint_as_float(fg[i][2]); }
      b0 = rbf(sum_mod4(b0)); b1 = rbf(sum_mod4(b1));
      const float g0 = rbf(b0 / (1.f + __expf(-b0))), g1 = rbf(b1 / (1.f + __expf(-b1)));
      if (lane < GPF) *(LAS f32x2*)(L + L_RED + 768 + 8 * lane) = f32x2{g0, g1};
      asm volatile("s_waitcnt lgkmcnt(0)" ::: "memory");
    }
    return;
  }

  const int cw = wave - 1, t = threadIdx.x - 64;

  const u32x2 ep = __builtin_amdgcn_raw_buffer_load_b64(ws, W_EPOCH, 0, 16);
  const u32x4 xr = *(const GAS u32x4*)(x + 8 * lane);
  unsigned xo = *(const GAS unsigned*)(x + 2 * GPF * fin + 2 * (lane % GPF));
  const int trow = 32 * cw + (lane >> 3);
  __builtin_amdgcn_s_barrier();

  const unsigned long long tag = (((unsigned long long)ep[1] << 32) | ep[0]) + 1ull;
  const unsigned tlo = __builtin_amdgcn_readfirstlane((unsigned)tag), thi = __builtin_amdgcn_readfirstlane((unsigned)(tag >> 32));

  {
    float xf[8];
#pragma unroll
    for (int k = 0; k < 4; ++k) { xf[2 * k] = bflo(xr[k]); xf[2 * k + 1] = bfhi(xr[k]); }
    float sum = ((xf[0] + xf[1]) + (xf[2] + xf[3])) + ((xf[4] + xf[5]) + (xf[6] + xf[7]));
    const float mean = wave_sum(sum) * (1.f / DM);
    float sq = 0.f;
#pragma unroll
    for (int k = 0; k < 8; ++k) { const float d = xf[k] - mean; sq = fmaf(d, d, sq); }
    const float var = wave_sum(sq) * (1.f / DM);
    const float mb = rbf(mean), sd = rbf(__builtin_amdgcn_sqrtf(rbf(rbf(var) + 1e-5f)));
    const float rs = __builtin_amdgcn_rcpf(sd);
    f32x4 a, b;
#pragma unroll
    for (int k = 0; k < 4; ++k) { a[k] = rbf(rbf(xf[k] - mb) * rs); b[k] = rbf(rbf(xf[4 + k] - mb) * rs); }
    *(LAS f32x4*)(L + L_XN + 32 * lane) = a;
    *(LAS f32x4*)(L + L_XN + 32 * lane + 16) = b;
  }
  asm volatile("s_waitcnt lgkmcnt(0)" : "+v"(xo) :: "memory");
  float xq[4];
#pragma unroll
  for (int i = 0; i < 4; ++i) xq[i] = *(const LAS float*)(L + L_XN + 4 * (128 * (s >> 3) + trow + 8 * i));
  for (unsigned spins = 0; LDSU(ctrl[1]) < 2u && spins < SPIN_CAP; ++spins) {}
  asm volatile("" ::: "memory");
  u32x4 wqr[4];
#pragma unroll
  for (int i = 0; i < 4; ++i) wqr[i] = *(const LAS u32x4*)(L + L_WO + (trow + 8 * i) * 128 + (lane & 7) * 16);

  LAS float* red = (LAS float*)(L + L_RED);
  {
    float acc[8];
#pragma unroll
    for (int k = 0; k < 8; ++k) acc[k] = 0.f;
#pragma unroll
    for (int i = 0; i < 4; ++i)
#pragma unroll
      for (int k = 0; k < 4; ++k) {
        acc[2 * k] = fmaf(xq[i], bflo(wqr[i][k]), acc[2 * k]);
        acc[2 * k + 1] = fmaf(xq[i], bfhi(wqr[i][k]), acc[2 * k + 1]);
      }
#pragma unroll
    for (int k = 0; k < 8; ++k) acc[k] = sum_mod8(acc[k]);
    if (lane < 8) {
      *(LAS f32x4*)(red + cw * 64 + 8 * lane) = f32x4{acc[0], acc[1], acc[2], acc[3]};
      *(LAS f32x4*)(red + cw * 64 + 8 * lane + 4) = f32x4{acc[4], acc[5], acc[6], acc[7]};
    }
  }
  asm volatile("s_waitcnt lgkmcnt(0)" ::: "memory");
  lds_count(ctrl + 8, lane);
  if (cw == 0) {
    for (unsigned spins = 0; LDSU(ctrl[8]) < 4u && spins < SPIN_CAP; ++spins) {}
    asm volatile("" ::: "memory");
    if (lane == 0) ctrl[3] = 2u;
    if (lane < 32) {
      const f32x2 q2 = (*(const LAS f32x2*)(red + 2 * lane) + *(const LAS f32x2*)(red + 64 + 2 * lane)) +
                       (*(const LAS f32x2*)(red + 128 + 2 * lane) + *(const LAS f32x2*)(red + 192 + 2 * lane));
      st_gran(ws, W_Q + (unsigned)(((g * 8 + (s & 7)) * 4 + (s >> 3)) * 32 + lane) * 16, __float_as_uint(q2[0]), tlo, __float_as_uint(q2[1]), thi);
    }
  }
  unsigned ka[4], va[8];
  {
    const unsigned rho = lane & 15, gg = lane >> 4;
    const unsigned ring0 = (unsigned)(size_t)(L + L_RING);
#pragma unroll
    for (int m = 0; m < 4; ++m) ka[m] = ring0 + rho * 256 + 16 * ((4 * m + gg) ^ rho);
    const unsigned q = (lane & 15) >> 2, p = lane & 3, row = 4 * gg + q;
#pragma unroll
    for (int b = 0; b < 8; ++b) va[b] = ring0 + 4096 + row * 256 + 8 * p + 32 * (b ^ (row & 7));
  }
  int slot = cw;
  auto fetch = [&](int k, bf16x8 (&kf)[4], bf16x4 (&vf)[8]) {
    const unsigned j = 4 * k + cw;
    for (unsigned spins = 0; LDSU(ctrl[0]) <= j && spins < SPIN_CAP; ++spins) __builtin_amdgcn_s_sleep(1);
    asm volatile("" ::: "memory");
    const unsigned sb = (unsigned)slot * SLOT;
    slot += 4; if (slot >= RING) slot -= RING;
#pragma unroll
    for (int m = 0; m < 4; ++m) kf[m] = *(const LAS bf16x8*)(size_t)(ka[m] + sb);
#pragma unroll
    for (int b = 0; b < 8; ++b) vf[b] = __builtin_amdgcn_ds_read_tr16_b64_v4bf16((LAS bf16x4*)(size_t)(va[b] + sb));
  };
  auto release = [&](int k) {
    asm volatile("s_waitcnt lgkmcnt(0)" ::: "memory");
    if (lane == 0) ctrl[4 + cw] = (unsigned)k + 1u;
  };
  bf16x8 kp[NPRE][4];
  bf16x4 vp[NPRE][8];
#pragma unroll
  for (int k = 0; k < NPRE; ++k) { fetch(k, kp[k], vp[k]); release(k); }
#pragma unroll
  for (int k = 0; k < NPRE; ++k) {
#pragma unroll
    for (int m = 0; m < 4; ++m) asm volatile("" : "+v"(kp[k][m]));
#pragma unroll
    for (int b = 0; b < 8; ++b) asm volatile("" : "+v"(vp[k][b]));
  }

  {
    u32x4 qa[4] = {};
    const unsigned off = W_Q + (unsigned)((g * 8 + (t >> 5)) * 128 + (t & 31)) * 16;
    bool ok[4] = {false, false, false, false};
    for (unsigned spins = 0; spins < SPIN_CAP; ++spins) {
      asm volatile("" ::: "memory");
#pragma unroll
      for (int i = 0; i < 4; ++i) poll_gran(ws, off + i * 512, ok[i], qa[i]);
#pragma unroll
      for (int i = 0; i < 4; ++i) ok[i] = qa[i][1] == tlo && qa[i][3] == thi;
      if (__all(ok[0] && ok[1] && ok[2] && ok[3])) break;
      poll_pause(!(ok[0] && ok[1] && ok[2] && ok[3]));
    }
    const float q0 = (__uint_as_float(qa[0][0]) + __uint_as_float(qa[1][0])) + (__uint_as_float(qa[2][0]) + __uint_as_float(qa[3][0]));
    const float q1 = (__uint_as_float(qa[0][2]) + __uint_as_float(qa[1][2])) + (__uint_as_float(qa[2][2]) + __uint_as_float(qa[3][2]));
    *(LAS unsigned*)(L + L_Q + 4 * t) = pack2(q0, q1);
    asm volatile("s_waitcnt lgkmcnt(0)" ::: "memory");
    lds_count(ctrl + 9, lane);
    for (unsigned spins = 0; LDSU(ctrl[9]) < 4u && spins < SPIN_CAP; ++spins) {}
  }
  asm volatile("" ::: "memory");
  bf16x8 qf[4];
  {
    const int col = lane & 15, gg = lane >> 4;
#pragma unroll
    for (int m = 0; m < 4; ++m) qf[m] = *(const LAS bf16x8*)(L + L_Q + (col & 3) * 256 + 64 * m + 16 * gg);
  }

  constexpr float CS = 0.08838834764831845f * 1.4426950408889634f;
  f32x4 acc[8], accl = {0.f, 0.f, 0.f, 0.f};
#pragma unroll
  for (int b = 0; b < 8; ++b) acc[b] = f32x4{0.f, 0.f, 0.f, 0.f};
  float mrun = -INFINITY, mc = 0.f;
  const s16x4 ones = {0x3F80, 0x3F80, 0x3F80, 0x3F80};
  auto scores = [&](const bf16x8 (&kf)[4]) -> s16x4 {
    f32x4 sc = {0.f, 0.f, 0.f, 0.f};
#pragma unroll
    for (int m = 0; m < 4; ++m) sc = __builtin_amdgcn_mfma_f32_16x16x32_bf16(kf[m], qf[m], sc, 0, 0, 0);
    const float mx = fmaxf(fmaxf(sc[0], sc[1]), fmaxf(sc[2], sc[3]));
    if (__any(mx > mrun)) {
      const float mn = fmaxf(mrun, xrow_max(mx));
      const float alpha = __builtin_amdgcn_exp2f((mrun - mn) * CS);
      mrun = mn; mc = mn * CS;
#pragma unroll
      for (int b = 0; b < 8; ++b) acc[b] *= alpha;
      accl *= alpha;
    }
    const float p0 = __builtin_amdgcn_exp2f(fmaf(sc[0], CS, -mc)), p1 = __builtin_amdgcn_exp2f(fmaf(sc[1], CS, -mc));
    const float p2 = __builtin_amdgcn_exp2f(fmaf(sc[2], CS, -mc)), p3 = __builtin_amdgcn_exp2f(fmaf(sc[3], CS, -mc));
    const u32x2 pp = {pack2(p0, p1), pack2(p2, p3)};
    return __builtin_bit_cast(s16x4, pp);
  };
  auto pv = [&](const bf16x4 (&vf)[8], s16x4 pf) {
    accl = __builtin_amdgcn_mfma_f32_16x16x16bf16_1k(ones, pf, accl, 0, 0, 0);
#pragma unroll
    for (int b = 0; b < 8; ++b) acc[b] = __builtin_amdgcn_mfma_f32_16x16x16bf16_1k(__builtin_bit_cast(s16x4, vf[b]), pf, acc[b], 0, 0, 0);
  };
#pragma unroll
  for (int k = 0; k < NPRE; ++k) pv(vp[k], scores(kp[k]));
  const int nk = (nfill - cw + 3) >> 2;
  for (int k = NPRE; k < nk - 2; ++k) {
    bf16x8 kf[4];
    bf16x4 vf[8];
    fetch(k, kf, vf);
    const s16x4 pf = scores(kf);
    release(k);
    pv(vf, pf);
  }
  for (unsigned spins = 0; LDSU(ctrl[1]) < (has_job ? 20u : 4u) && spins < SPIN_CAP; ++spins) __builtin_amdgcn_s_sleep(1);
  asm volatile("" ::: "memory");
  f32x2 wof[16];
#pragma unroll
  for (int r = 0; r < 16; ++r) {
    const unsigned u = *(const LAS unsigned*)(L + L_WO + r * 1024 + 4 * t);
    wof[r] = f32x2{bflo(u), bfhi(u)};
  }
#pragma unroll
  for (int r = 0; r < 16; ++r) asm volatile("" : "+v"(wof[r]));
  unsigned o1off = W_O + (unsigned)((((g * 4 + cw) * 8 + (lane >> 3)) * 32 + s) * 8 + (lane & 7)) * 16;
  unsigned ooff = W_O + (unsigned)(((g * 4 + (s >> 3)) * 8 + (s & 7)) * 256 + t) * 16;
  unsigned soff_out = W_S + (unsigned)(((t / GPF) * 256 + c) * GPF + t % GPF) * 16;
  unsigned mgw = (unsigned)(size_t)(L + L_MRG) + 4 * ((cw * 4 + (lane & 3)) * 128 + 32 * ((lane >> 2) & 3) + 4 * (lane >> 4));
  unsigned mgr = (unsigned)(size_t)(L + L_MRG) + 4 * (cw * 128 + 2 * lane);
  asm volatile("" : "+v"(o1off), "+v"(ooff), "+v"(soff_out), "+v"(mgw), "+v"(mgr));
  for (int k = nk - 2; k < nk; ++k) {
    bf16x8 kf[4];
    bf16x4 vf[8];
    fetch(k, kf, vf);
    const s16x4 pf = scores(kf);
    release(k);
    pv(vf, pf);
  }

  {
    const bool r1 = lane & 4, r2 = lane & 8;
    f32x4 va0, va1;
#pragma unroll
    for (int e = 0; e < 4; ++e) {
      const float a = r1 ? acc[2][e] : acc[0][e], b = r1 ? acc[6][e] : acc[4][e];
      const float c2 = r1 ? acc[3][e] : acc[1][e], d = r1 ? acc[7][e] : acc[5][e];
      va0[e] = r2 ? b : a; va1[e] = r2 ? d : c2;
    }
    *(LAS f32x4*)(size_t)(mgw) = va0;
    *(LAS f32x4*)(size_t)(mgw + 64) = va1;
    if (lane < 4) *(LAS f32x2*)(L + L_ML + (lane * 4 + cw) * 8) = f32x2{mc, accl[0]};
  }
  asm volatile("s_waitcnt lgkmcnt(0)" ::: "memory");
  lds_count(ctrl + 12, lane);
  for (unsigned spins = 0; LDSU(ctrl[12]) < 4u && spins < SPIN_CAP; ++spins) {}
  asm volatile("" ::: "memory");
  {
    const unsigned tb = thi & 0xffff0000u;
    const int head = cw;
    const f32x4 m01 = *(const LAS f32x4*)(L + L_ML + head * 32), m23 = *(const LAS f32x4*)(L + L_ML + head * 32 + 16);
    const f32x2 o0 = *(const LAS f32x2*)(size_t)(mgr), o1 = *(const LAS f32x2*)(size_t)(mgr + 2048);
    const f32x2 o2 = *(const LAS f32x2*)(size_t)(mgr + 4096), o3 = *(const LAS f32x2*)(size_t)(mgr + 6144);
    const float M = fmaxf(fmaxf(m01[0], m01[2]), fmaxf(m23[0], m23[2]));
    const float f0 = __builtin_amdgcn_exp2f(m01[0] - M), f1 = __builtin_amdgcn_exp2f(m01[2] - M);
    const float f2 = __builtin_amdgcn_exp2f(m23[0] - M), f3 = __builtin_amdgcn_exp2f(m23[2] - M);
    const float Ls = fmaf(f0, m01[1], f1 * m01[3]) + fmaf(f2, m23[1], f3 * m23[3]);
    const float rl = __builtin_amdgcn_rcpf(Ls);
    const f32x2 o = (f0 * o0 + f1 * o1 + (f2 * o2 + f3 * o3)) * rl;
    const unsigned lam = __float_as_uint(M + __builtin_amdgcn_logf(Ls));
    st_gran(ws, o1off, __float_as_uint(o[0]), tlo, __float_as_uint(o[1]), tb | ((lane & 1) ? (lam & 0xffffu) : (lam >> 16)));
  }

  u32x4 sg[GPF] = {};
  const unsigned soff = W_S + (unsigned)((fin * 256 + t / GPF) * GPF + t % GPF) * 16;

  {
    const unsigned tb = thi & 0xffff0000u;
    u32x4 og = {};
    bool ok = false;
    __builtin_amdgcn_s_sleep(DELAY1);
    for (unsigned spins = 0; spins < SPIN_CAP; ++spins) {
      asm volatile("" ::: "memory");
      poll_gran(ws, ooff, ok, og);
      ok = og[1] == tlo && (og[3] & 0xffff0000u) == tb;
      if (__all(ok)) break;
      poll_pause(!ok);
    }
    const unsigned mine = og[3] & 0xffffu, other = dppu<XOR1>(mine);
    const float lam = __uint_as_float((lane & 1) ? ((other << 16) | mine) : ((mine << 16) | other));
    const float Mw = max_mod8(lam);
    const float wt = __builtin_amdgcn_exp2f(lam - Mw);
    const float a0 = sum_mod8(wt * __uint_as_float(og[0])), a1 = sum_mod8(wt * __uint_as_float(og[2])), Ww = sum_mod8(wt);
    LAS float* r2 = (LAS float*)(L + L_RED);
    if (lane < 8) { r2[(2 * lane) * 4 + cw] = a0; r2[(2 * lane + 1) * 4 + cw] = a1; }
    if (lane == 8) { r2[64 + cw] = Mw; r2[68 + cw] = Ww; }
    asm volatile("s_waitcnt lgkmcnt(0)" ::: "memory");
    __builtin_amdgcn_s_barrier();
    const f32x4 ov = *(const LAS f32x4*)(r2 + 4 * (lane & 15)), mv = *(const LAS f32x4*)(r2 + 64), wv = *(const LAS f32x4*)(r2 + 68);
    const float M = fmaxf(fmaxf(mv[0], mv[1]), fmaxf(mv[2], mv[3]));
    const float f0 = __builtin_amdgcn_exp2f(mv[0] - M), f1 = __builtin_amdgcn_exp2f(mv[1] - M);
    const float f2 = __builtin_amdgcn_exp2f(mv[2] - M), f3 = __builtin_amdgcn_exp2f(mv[3] - M);
    const float num = fmaf(f0, ov[0], f1 * ov[1]) + fmaf(f2, ov[2], f3 * ov[3]);
    const float den = fmaf(f0, wv[0], f1 * wv[1]) + fmaf(f2, wv[2], f3 * wv[3]);
    const float od = rbf(num * __builtin_amdgcn_rcpf(den));
    f32x2 sa = {0.f, 0.f}, sb = {0.f, 0.f};
#pragma unroll
    for (int r = 0; r < 16; r += 2) {
      const float oa = __builtin_bit_cast(float, __builtin_amdgcn_readlane(__builtin_bit_cast(int, od), r));
      const float ob = __builtin_bit_cast(float, __builtin_amdgcn_readlane(__builtin_bit_cast(int, od), r + 1));
      sa += oa * wof[r]; sb += ob * wof[r + 1];
    }
    const f32x2 sv = sa + sb;
    st_gran(ws, soff_out, __float_as_uint(sv[0]), tlo, __float_as_uint(sv[1]), thi);
  }

  if (is_fin) {
    bool sok[GPF];
#pragma unroll
    for (int i = 0; i < GPF; ++i) sok[i] = false;
    __builtin_amdgcn_s_sleep(DELAY2);
    for (unsigned spins = 0; spins < SPIN_CAP; ++spins) {
      asm volatile("" ::: "memory");
      bool all = true;
#pragma unroll
      for (int i = 0; i < GPF; ++i) poll_gran(ws, soff + i * 4096, sok[i], sg[i]);
#pragma unroll
      for (int i = 0; i < GPF; ++i) { sok[i] = sg[i][1] == tlo && sg[i][3] == thi; all = all && sok[i]; }
      if (__all(all)) break;
      poll_pause(!all);
    }
    float a0 = 0.f, a1 = 0.f;
#pragma unroll
    for (int i = 0; i < GPF; i += 2) {
      a0 += __uint_as_float(sg[i][0]) + __uint_as_float(sg[i + 1][0]);
      a1 += __uint_as_float(sg[i][2]) + __uint_as_float(sg[i + 1][2]);
    }
    a0 = sum_mod4(a0); a1 = sum_mod4(a1);
    LAS float* r3 = (LAS float*)(L + L_RED) + 96;
    if (lane < GPF) { r3[(2 * lane) * 4 + cw] = a0; r3[(2 * lane + 1) * 4 + cw] = a1; }
    asm volatile("s_waitcnt lgkmcnt(0)" ::: "memory");
    __builtin_amdgcn_s_barrier();
    if (t < GPF) {
      const f32x4 u0 = *(const LAS f32x4*)(r3 + 8 * t), u1 = *(const LAS f32x4*)(r3 + 8 * t + 4);
      const f32x2 gv = *(const LAS f32x2*)(L + L_RED + 768 + 8 * t);
      const float s0 = rbf((u0[0] + u0[1]) + (u0[2] + u0[3])), s1 = rbf((u1[0] + u1[1]) + (u1[2] + u1[3]));
      unsigned res = pack2(bflo(xo) + rbf(s0 * gv[0]), bfhi(xo) + rbf(s1 * gv[1]));
      *(GAS unsigned*)(out + 2 * GPF * fin + 2 * t) = res;
      if (fin == 0 && t == 0) __builtin_amdgcn_raw_buffer_store_b64(u32x2{tlo, thi}, ws, W_EPOCH, 0, 0);
    }
  }
}

}

extern "C" void kernel_launch(void* const* d_in, const int* in_sizes, int n_in, void* d_out, int out_size, void* d_ws, size_t ws_size,
                              hipStream_t stream) {
  (void)in_sizes; (void)n_in; (void)out_size; (void)ws_size;
  (void)hipFuncSetAttribute((const void*)fused_decode_kernel, hipFuncAttributeMaxDynamicSharedMemorySize, L_END);
  hipLaunchKernelGGL(fused_decode_kernel, dim3(NWG), dim3(NTHR), L_END, stream,
                     (const unsigned short*)d_in[0], (const unsigned short*)d_in[1], (const unsigned short*)d_in[2],
                     (const unsigned short*)d_in[3], (const unsigned short*)d_in[4], (const unsigned short*)d_in[6],
                     (const unsigned short*)d_in[7], (unsigned short*)d_out, (unsigned char*)d_ws);
}
```
